```python
import torch

def _softmax(x, axis=-1):
    return torch.softmax(x, dim=axis)
def _gelu(x):
    return torch.nn.functional.gelu(x, approximate="tanh")
def _silu(x):
    return torch.nn.functional.silu(x)
def _sigmoid(x):
    return torch.sigmoid(x)
def _layernorm(x, axis=-1, eps=1e-5):
    m = x.mean(dim=axis, keepdim=True)
    v = x.var(dim=axis, keepdim=True, unbiased=False)
    return (x - m) / torch.sqrt(v + eps)
def _scatter_add(tgt, idx, src, axis=0):
    return tgt.clone().index_add_(axis, idx, src)
def _segment_sum(src, seg, n):
    out = torch.zeros((n,) + src.shape[1:], dtype=src.dtype)
    return out.index_add_(0, seg, src)
def _pad(x, pw):
    flat = tuple(v for pair in reversed(pw) for v in pair)
    return torch.nn.functional.pad(x, flat)

kernel_name = 'parallel_sum__ssm_conv_s5__glu_gated_swiglu__prompt'

def setup_inputs():

    _w = (lambda a: (lambda t: t.to(torch.bfloat16) if t.is_floating_point() else t)(torch.as_tensor(a)))
    def _A_setup():
        B, S, D = 1, 2048, 512
        P = D
        Lambda_re = -0.5 * (lambda _x: torch.exp(torch.as_tensor(_x)))((lambda *s: torch.randn(*s, dtype=torch.bfloat16) * 0.2)(P))
        Lambda_im = (lambda *s: torch.randn(*s, dtype=torch.bfloat16) * 0.8)(P) * 1.0
        B_re = (lambda *s: torch.randn(*s, dtype=torch.bfloat16) * 0.2)(P, D) * (D ** -0.5)
        B_im = (lambda *s: torch.randn(*s, dtype=torch.bfloat16) * 0.2)(P, D) * (D ** -0.5)
        C_re = (lambda *s: torch.randn(*s, dtype=torch.bfloat16) * 0.2)(D, P) * (P ** -0.5)
        C_im = (lambda *s: torch.randn(*s, dtype=torch.bfloat16) * 0.2)(D, P) * (P ** -0.5)
        log_dt = (lambda *s: torch.randn(*s, dtype=torch.bfloat16) * 0.2)(P) * 0.3 - 2.0
        Dskip = (lambda *s: torch.randn(*s, dtype=torch.bfloat16) * 0.05)(D)
        return {
            "x": _w((lambda *s: torch.randn(*s, dtype=torch.bfloat16) * 0.05)(B, S, D)),
            "Lambda_re": _w(Lambda_re),
            "Lambda_im": _w(Lambda_im),
            "B_re": _w(B_re),
            "B_im": _w(B_im),
            "C_re": _w(C_re),
            "C_im": _w(C_im),
            "log_dt": _w(log_dt),
            "Dskip": _w(Dskip),
        }
    _A_in = _A_setup()

    def _B_setup():
        B, S, D, F = 1, 2048, 512, 1792
        return {
            "x": _w((lambda *s: torch.randn(*s, dtype=torch.bfloat16) * 0.05)(B, S, D)),
            "w1": _w((lambda *s: torch.randn(*s, dtype=torch.bfloat16) * 0.4)(D, F) * (2.0 / D) ** 0.5),
            "w3": _w((lambda *s: torch.randn(*s, dtype=torch.bfloat16) * 0.4)(D, F) * (2.0 / D) ** 0.5),
            "w2": _w((lambda *s: torch.randn(*s, dtype=torch.bfloat16) * 0.4)(F, D) * (2.0 / F) ** 0.5),
        }
    _B_in = _B_setup()

    return {
        "A_x": _A_in["x"],
        "A_Lambda_re": _A_in["Lambda_re"],
        "A_Lambda_im": _A_in["Lambda_im"],
        "A_B_re": _A_in["B_re"],
        "A_B_im": _A_in["B_im"],
        "A_C_re": _A_in["C_re"],
        "A_C_im": _A_in["C_im"],
        "A_log_dt": _A_in["log_dt"],
        "A_Dskip": _A_in["Dskip"],
        "B_x": _B_in["x"],
        "B_w1": _B_in["w1"],
        "B_w3": _B_in["w3"],
        "B_w2": _B_in["w2"],
    }

def reference(A_x, A_Lambda_re, A_Lambda_im, A_B_re, A_B_im, A_C_re, A_C_im, A_log_dt, A_Dskip, B_x, B_w1, B_w3, B_w2):
    def _A_ref(x, Lambda_re, Lambda_im, B_re, B_im, C_re, C_im, log_dt, Dskip):
        Bsz, L, H = x.shape
        Lam = Lambda_re + 1j * Lambda_im
        Bmat = B_re + 1j * B_im
        Cmat = C_re + 1j * C_im
        dt = torch.exp(log_dt)
        Lam_bar = torch.exp(dt * Lam)
        B_bar = ((Lam_bar - 1.0) / Lam)[:, None] * Bmat
        Bu = torch.einsum("bld,pd->blp", x + 0j, B_bar)
        h = torch.zeros_like(Bu[:, 0, :])
        hs = []
        for t in range(L):
            h = Lam_bar[None, :] * h + Bu[:, t, :]
            hs.append(h)
        hseq = torch.stack(hs, 1)
        y = 2.0 * torch.einsum("blp,dp->bld", hseq, Cmat).real + x * Dskip[None, None, :]
        return y

    def _B_ref(x, w1, w3, w2):
        gate = torch.einsum("bsd,df->bsf", x, w1)
        up = torch.einsum("bsd,df->bsf", x, w3)
        h = _silu(gate) * up
        out = torch.einsum("bsf,fd->bsd", h, w2)
        return out

    x = A_x
    xn = _layernorm(x, axis=-1)
    return x + _A_ref(xn, A_Lambda_re, A_Lambda_im, A_B_re, A_B_im, A_C_re, A_C_im, A_log_dt, A_Dskip) + _B_ref(xn, B_w1, B_w3, B_w2)
```

```cpp
#include <hip/hip_runtime.h>
#include <hip/hip_bf16.h>
#include <stdint.h>

typedef unsigned short u16;
__device__ __forceinline__ float bf2f(u16 v) { return __uint_as_float(((unsigned)v) << 16); }
__device__ __forceinline__ u16 f2bf(float f) { __hip_bfloat16 b = __float2bfloat16(f); return *(u16*)&b; }

constexpr int L = 2048, D = 512, P = 512, F = 1792;

__global__ void k_ln(const u16* x, float* xn) {
  int row = blockIdx.x; int t = threadIdx.x;
  float v[8]; float s = 0;
  for (int i = 0; i < 8; i++) { v[i] = bf2f(x[row * D + t * 8 + i]); s += v[i]; }
  for (int o = 32; o; o >>= 1) s += __shfl_xor(s, o);
  float m = s / D; float q = 0;
  for (int i = 0; i < 8; i++) { float d = v[i] - m; q += d * d; }
  for (int o = 32; o; o >>= 1) q += __shfl_xor(q, o);
  float r = rsqrtf(q / D + 1e-5f);
  for (int i = 0; i < 8; i++) xn[row * D + t * 8 + i] = bf2f(f2bf((v[i] - m) * r));
}
__global__ void k_bu(const float* xn, const u16* Bre, const u16* Bim, float* Ure, float* Uim) {
  int p = blockIdx.x * 64 + threadIdx.x; int l = blockIdx.y;
  float a = 0, b = 0;
  for (int d = 0; d < D; d++) { float xv = xn[l * D + d]; a += xv * bf2f(Bre[p * D + d]); b += xv * bf2f(Bim[p * D + d]); }
  Ure[l * P + p] = a; Uim[l * P + p] = b;
}
__global__ void k_scan(const u16* Lre, const u16* Lim, const u16* logdt, float* Ure, float* Uim) {
  int p = blockIdx.x * 64 + threadIdx.x;
  float lr = bf2f(Lre[p]), li = bf2f(Lim[p]);
  float dt = bf2f(f2bf(expf(bf2f(logdt[p]))));
  float er = expf(dt * lr); float ar = er * cosf(dt * li), ai = er * sinf(dt * li);
  float nr = ar - 1.f, ni = ai; float den = lr * lr + li * li;
  float sr = (nr * lr + ni * li) / den, si = (ni * lr - nr * li) / den;
  float hr = 0, hi = 0;
  for (int l = 0; l < L; l++) {
    float ur = Ure[l * P + p], ui = Uim[l * P + p];
    float br = sr * ur - si * ui, bi = sr * ui + si * ur;
    float nhr = ar * hr - ai * hi + br, nhi = ar * hi + ai * hr + bi;
    hr = nhr; hi = nhi;
    Ure[l * P + p] = hr; Uim[l * P + p] = hi;
  }
}
__global__ void k_ffn1(const float* xn, const u16* w1, const u16* w3, float* h) {
  int f = blockIdx.x * 64 + threadIdx.x; int l = blockIdx.y;
  float a = 0, b = 0;
  for (int d = 0; d < D; d++) { float xv = xn[l * D + d]; a += xv * bf2f(w1[d * F + f]); b += xv * bf2f(w3[d * F + f]); }
  h[l * F + f] = a / (1.f + expf(-a)) * b;
}
__global__ void k_out(const u16* x, const float* xn, const float* hre, const float* him, const float* hf,
                      const u16* Cre, const u16* Cim, const u16* Dsk, const u16* w2, float* out) {
  int d = blockIdx.x * 64 + threadIdx.x; int l = blockIdx.y;
  float a = 0;
  for (int p = 0; p < P; p++) a += hre[l * P + p] * bf2f(Cre[d * P + p]) - him[l * P + p] * bf2f(Cim[d * P + p]);
  float b = 0;
  for (int f = 0; f < F; f++) b += hf[l * F + f] * bf2f(w2[f * D + d]);
  out[l * D + d] = bf2f(x[l * D + d]) + 2.f * a + xn[l * D + d] * bf2f(Dsk[d]) + b;
}

extern "C" void kernel_launch(void* const* d_in, const int* in_sizes, int n_in,
                              void* d_out, int out_size, void* d_ws, size_t ws_size,
                              hipStream_t stream) {
  const u16* x = (const u16*)d_in[0];
  const u16* Lre = (const u16*)d_in[1];
  const u16* Lim = (const u16*)d_in[2];
  const u16* Bre = (const u16*)d_in[3];
  const u16* Bim = (const u16*)d_in[4];
  const u16* Cre = (const u16*)d_in[5];
  const u16* Cim = (const u16*)d_in[6];
  const u16* logdt = (const u16*)d_in[7];
  const u16* Dsk = (const u16*)d_in[8];
  const u16* w1 = (const u16*)d_in[10];
  const u16* w3 = (const u16*)d_in[11];
  const u16* w2 = (const u16*)d_in[12];
  float* ws = (float*)d_ws;
  float* xn = ws; float* Ure = xn + L * D; float* Uim = Ure + L * P; float* hf = Uim + L * P;
  k_ln<<<L, 64, 0, stream>>>(x, xn);
  k_bu<<<dim3(P / 64, L), 64, 0, stream>>>(xn, Bre, Bim, Ure, Uim);
  k_scan<<<P / 64, 64, 0, stream>>>(Lre, Lim, logdt, Ure, Uim);
  k_ffn1<<<dim3(F / 64, L), 64, 0, stream>>>(xn, w1, w3, hf);
  k_out<<<dim3(D / 64, L), 64, 0, stream>>>(x, xn, Ure, Uim, hf, Cre, Cim, Dsk, w2, (float*)d_out);
}
```

```cpp
#include <hip/hip_runtime.h>
#include <stdint.h>

typedef unsigned short u16;
typedef unsigned int u32;
typedef unsigned long long u64;
typedef __attribute__((ext_vector_type(8))) __bf16 bf16x8;
typedef __attribute__((ext_vector_type(4))) __bf16 bf16x4;
typedef __attribute__((ext_vector_type(2))) __bf16 bf16x2;
typedef __attribute__((ext_vector_type(16))) float f32x16;
typedef __attribute__((ext_vector_type(4))) float f32x4;
typedef __attribute__((ext_vector_type(2))) float f32x2;
typedef __attribute__((ext_vector_type(4))) u32 v4u;
typedef __attribute__((ext_vector_type(2))) u32 v2u;
#define LAS __attribute__((address_space(3)))
#define DEVI __device__ __forceinline__

constexpr int L = 2048, D = 512, P = 512, F = 1792;

constexpr u32 WS_EPOCH = 0;
constexpr u32 WS_AGG = 4096;
constexpr u32 WS_STAT = WS_AGG + 32 * 512 * 16;
constexpr u32 WS_STAMP = 512 * 1024;
constexpr u32 WS_H = 1u << 20;
constexpr u32 H_BYTES = 64u * 176u * 1024u;
constexpr u32 WS_BYTES = WS_H + H_BYTES;

constexpr u32 XO = 0, BSO = 16384, W1O = 24576, W3O = 24576 + 14336 + 128, SLOT = W3O + 14336;
constexpr u32 COLP = 3 * SLOT;
constexpr u32 ROWP = 2 * SLOT + 49152;
constexpr u32 SCR = 0;
constexpr u32 LDS_BYTES = 163840;

struct Params {
  const u16 *x, *lre, *lim, *bre, *bim, *cre, *cim, *ldt, *dsk, *w1, *w3, *w2;
  float* out;
  char* ws;
};

DEVI v4u make_srd(const void* p, u32 bytes) {
  u64 a = (u64)p;
  return v4u{(u32)a, (u32)(a >> 32) & 0xffffu, bytes, 0x00020000u};
}
DEVI void glds16(u32 lds, u32 voff, v4u srd, u32 soff) {
  asm volatile("s_mov_b32 m0, %0\n\ts_nop 0\n\tbuffer_load_dwordx4 %1, %2, %3 offen lds" ::"s"(lds), "v"(voff), "s"(srd), "s"(soff)
               : "memory");
}
template <int N> DEVI void wait_vm_bar() {
  asm volatile("s_waitcnt vmcnt(%0) lgkmcnt(0)\n\ts_barrier" ::"n"(N) : "memory");
}
template <int N> DEVI void wait_vm() { asm volatile("s_waitcnt vmcnt(%0)" ::"n"(N) : "memory"); }
DEVI void wait_lgkm() { asm volatile("s_waitcnt lgkmcnt(0)" ::: "memory"); }
DEVI float bf2f(u32 v) { return __uint_as_float(v << 16); }
DEVI bf16x8 lds_r128(u32 a) { return *(LAS bf16x8*)a; }
DEVI f32x4 lds_rf4(u32 a) { return *(LAS f32x4*)a; }
DEVI bf16x8 lds_tr2(u32 a0, u32 a1) {
  bf16x4 lo = __builtin_amdgcn_ds_read_tr16_b64_v4bf16((LAS bf16x4*)a0);
  bf16x4 hi = __builtin_amdgcn_ds_read_tr16_b64_v4bf16((LAS bf16x4*)a1);
  return __builtin_shufflevector(lo, hi, 0, 1, 2, 3, 4, 5, 6, 7);
}
DEVI f32x16 mfma(bf16x8 a, bf16x8 b, f32x16 c) { return __builtin_amdgcn_mfma_f32_32x32x16_bf16(a, b, c, 0, 0, 0); }
DEVI float dot_ones(bf16x8 a, float c) {
  const bf16x2 o = {(__bf16)1.0f, (__bf16)1.0f};
  c = __builtin_amdgcn_fdot2_f32_bf16(__builtin_shufflevector(a, a, 0, 1), o, c, false);
  c = __builtin_amdgcn_fdot2_f32_bf16(__builtin_shufflevector(a, a, 2, 3), o, c, false);
  c = __builtin_amdgcn_fdot2_f32_bf16(__builtin_shufflevector(a, a, 4, 5), o, c, false);
  c = __builtin_amdgcn_fdot2_f32_bf16(__builtin_shufflevector(a, a, 6, 7), o, c, false);
  return c;
}
DEVI float dot_self(bf16x8 a, float c) {
  c = __builtin_amdgcn_fdot2_f32_bf16(__builtin_shufflevector(a, a, 0, 1), __builtin_shufflevector(a, a, 0, 1), c, false);
  c = __builtin_amdgcn_fdot2_f32_bf16(__builtin_shufflevector(a, a, 2, 3), __builtin_shufflevector(a, a, 2, 3), c, false);
  c = __builtin_amdgcn_fdot2_f32_bf16(__builtin_shufflevector(a, a, 4, 5), __builtin_shufflevector(a, a, 4, 5), c, false);
  c = __builtin_amdgcn_fdot2_f32_bf16(__builtin_shufflevector(a, a, 6, 7), __builtin_shufflevector(a, a, 6, 7), c, false);
  return c;
}
DEVI float half_sum(float v) {
  auto r = __builtin_amdgcn_permlane32_swap(__float_as_uint(v), __float_as_uint(v), false, false);
  return __uint_as_float(r[0]) + __uint_as_float(r[1]);
}
DEVI void half_both(float v, float& lo, float& hi) {
  auto r = __builtin_amdgcn_permlane32_swap(__float_as_uint(v), __float_as_uint(v), false, false);
  lo = __uint_as_float(r[0]);
  hi = __uint_as_float(r[1]);
}
DEVI u32 pk2(float a, float b) {
  bf16x2 v = {(__bf16)a, (__bf16)b};
  return __builtin_bit_cast(u32, v);
}
DEVI int tau(int r) { return 16 * ((r >> 2) & 1) + 4 * (r >> 3) + (r & 3); }
struct cf { float r, i; };
DEVI cf cmul(cf a, cf b) { return cf{a.r * b.r - a.i * b.i, a.r * b.i + a.i * b.r}; }
DEVI cf cfma(cf a, cf b, cf c) {
  return cf{__builtin_fmaf(a.r, b.r, __builtin_fmaf(-a.i, b.i, c.r)), __builtin_fmaf(a.r, b.i, __builtin_fmaf(a.i, b.r, c.i))};
}

#define STAMP(i) do {} while (0)
#define CSTAMP(i) do {} while (0)
#define STAMP_DECL
#define STAMP_ARG
#define STAMP_PASS
#define STAMP_FLUSH(lo, hi) do {} while (0)

template <int NW> struct Frag { bf16x8 x[2]; bf16x8 wv[NW]; };

template <int ROLE>
DEVI void phase1(const Params& p, const int w, const int lane, const int rp, const int cg STAMP_ARG) {
  constexpr bool SSM = ROLE == 0, EXTRA = (ROLE & 1) != 0;
  constexpr int NW = EXTRA ? 3 : 2;
  constexpr int NG = EXTRA ? 6 : 7;
  constexpr int J0 = SSM ? 0 : 2 * (ROLE - 1);
  const int mh = (w >> 1) & 1;
  const int l31 = lane & 31, h = lane >> 5;

  const v4u sx = make_srd(p.x, L * D * 2);
  const v4u sb = make_srd((w >> 2) ? p.bim : p.bre, P * D * 2);
  const v4u s1 = make_srd(p.w1, D * F * 2);
  const v4u s3 = make_srd(p.w3, D * F * 2);
  const v4u s13 = make_srd(w < 6 ? p.w1 : p.w3, D * F * 2);
  const v4u sws = make_srd(p.ws, WS_BYTES);

  u32 plr = 0, pli = 0, pld = 0;
  v2u pe = {0, 0};
  if constexpr (SSM) {
    const u32 co = (cg * 32 + l31) * 2;
    const v4u sa = make_srd(p.lre, P * 2), sc = make_srd(p.lim, P * 2), sd = make_srd(p.ldt, P * 2);
    const u32 eo = WS_EPOCH + cg * 256;
    asm volatile("buffer_load_ushort %0, %4, %5, 0 offen\n\tbuffer_load_ushort %1, %4, %6, 0 offen\n\t"
                 "buffer_load_ushort %2, %4, %7, 0 offen\n\tbuffer_load_dwordx2 %3, off, %8, %9 sc1"
                 : "=&v"(plr), "=&v"(pli), "=&v"(pld), "=&v"(pe)
                 : "v"(co), "s"(sa), "s"(sc), "s"(sd), "s"(sws), "s"(eo)
                 : "memory");
  }

  u32 vx[2], vb, vw[4];
#pragma unroll
  for (int e = 0; e < 2; e++) {
    const int R = 8 * (2 * w + e) + (lane >> 3);
    vx[e] = (u32)((rp * 128 + (R & ~31) + tau(R & 31)) * 1024 + (((lane & 7) ^ ((R >> 1) & 7)) << 4));
  }
  {
    const int R = 8 * w + (lane >> 3);
    vb = (u32)((cg * 32 + (R & 31)) * 1024 + (((lane & 7) ^ ((R >> 1) & 7)) << 4));
  }
  u32 wdst[4];
#pragma unroll
  for (int s = 0; s < 4; s++) {
    const int i = w + 8 * s;
    const int mat = i >= 14, idx = i - 14 * mat;
    const int ci = idx * 64 + lane;
    const int kr = ci / 14, cj = ci - kr * 14;
    vw[s] = (u32)(kr * (F * 2) + cj * 16 + cg * 224);
    wdst[s] = (mat ? W3O : W1O) + idx * 1024;
  }
  auto issue = [&](int st, u32 sbase) {
    const u32 so = st * 128, sw = st * (64 * F * 2);
    glds16(sbase + XO + (2 * w) * 1024, vx[0], sx, so);
    glds16(sbase + XO + (2 * w + 1) * 1024, vx[1], sx, so);
    glds16(sbase + wdst[0], vw[0], s1, sw);
    glds16(sbase + wdst[1], vw[1], s13, sw);
    glds16(sbase + wdst[2], vw[2], s3, sw);
    if constexpr (!EXTRA) glds16(sbase + wdst[3], vw[3], s3, sw);
    glds16(sbase + BSO + w * 1024, vb, sb, so);
  };
  issue(0, 0);
  issue(1, SLOT);
  issue(2, 2 * SLOT);

  u32 xa[4];
#pragma unroll
  for (int ks = 0; ks < 4; ks++) {
    const int kk = ks ^ (2 * mh);
    xa[ks] = (u32)(l31 * 128 + (((2 * kk + h) ^ ((l31 >> 1) & 7)) << 4));
  }
  u32 wa[2];
  {
    const int g1 = (lane >> 4) & 1, a = (lane & 15) >> 2, pp = lane & 3;
    const u32 base = ((pp >> 1) ? W3O : W1O) + (8 * h + a) * 224 + (8 * g1 + 4 * (pp & 1)) * 2;
    wa[0] = base + (mh ? 32 * 224 : 0);
    wa[1] = base + (mh ? 0 : 32 * 224);
  }
  constexpr u32 X0 = ROLE == 3 ? 4096 : 0, X1 = ROLE == 3 ? 0 : 4096;
  auto load = [&](Frag<NW>& f, u32 sbase, int ks) {
    const u32 ax = sbase + xa[ks] + mh * 8192;
    f.x[0] = lds_r128(ax + X0);
    f.x[1] = lds_r128(ax + X1);
    if constexpr (SSM) {
      const u32 ab = sbase + xa[ks] + BSO;
      f.wv[0] = lds_r128(ab);
      f.wv[1] = lds_r128(ab + 4096);
    } else {
      const u32 aw = sbase + wa[ks >> 1] + (ks & 1) * 16 * 224;
      f.wv[0] = lds_tr2(aw + J0 * 32, aw + J0 * 32 + 896);
      f.wv[1] = lds_tr2(aw + J0 * 32 + 32, aw + J0 * 32 + 32 + 896);
      if constexpr (EXTRA) f.wv[2] = lds_tr2(aw + 6 * 32, aw + 6 * 32 + 896);
    }
  };

  f32x16 acc[2][2], accx;
#pragma unroll
  for (int i = 0; i < 16; i++) { acc[0][0][i] = 0.f; acc[0][1][i] = 0.f; acc[1][0][i] = 0.f; acc[1][1][i] = 0.f; accx[i] = 0.f; }
  float rs[2] = {0.f, 0.f}, rq[2] = {0.f, 0.f}, cs[3] = {0.f, 0.f, 0.f};

  auto mma = [&](const Frag<NW>& f) {
    if constexpr (SSM) {
      acc[0][0] = mfma(f.x[0], f.wv[0], acc[0][0]);
      acc[0][1] = mfma(f.x[0], f.wv[1], acc[0][1]);
      acc[1][0] = mfma(f.x[1], f.wv[0], acc[1][0]);
      acc[1][1] = mfma(f.x[1], f.wv[1], acc[1][1]);
    } else {
      acc[0][0] = mfma(f.wv[0], f.x[0], acc[0][0]);
      acc[1][0] = mfma(f.wv[0], f.x[1], acc[1][0]);
      acc[0][1] = mfma(f.wv[1], f.x[0], acc[0][1]);
      acc[1][1] = mfma(f.wv[1], f.x[1], acc[1][1]);
      if constexpr (EXTRA) accx = mfma(f.wv[2], f.x[0], accx);
    }
  };
  auto stats = [&](const Frag<NW>& f, int ks) {
    if (ks == ROLE) {
      rs[0] = dot_ones(f.x[0], rs[0]); rq[0] = dot_self(f.x[0], rq[0]);
      rs[1] = dot_ones(f.x[1], rs[1]); rq[1] = dot_self(f.x[1], rq[1]);
    }
    if (ks < 2) { cs[0] = dot_ones(f.wv[0], cs[0]); cs[1] = dot_ones(f.wv[1], cs[1]); }
    if constexpr (EXTRA) { if (ks == (ROLE >> 1)) cs[2] = dot_ones(f.wv[2], cs[2]); }
  };

  STAMP(1);
  wait_vm_bar<2 * NG>();
  STAMP(2);
  cf lam = {0.f, 0.f}, s2 = {0.f, 0.f};
  u64 tag = 0;
  if constexpr (SSM) {
    asm volatile("" : "+v"(plr), "+v"(pli), "+v"(pld), "+v"(pe));
    const float lr = bf2f(plr), li = bf2f(pli);
    const float dt = (float)(__bf16)expf(bf2f(pld));
    const float er = expf(dt * lr);
    float sn, cn;
    sincosf(dt * li, &sn, &cn);
    lam = cf{er * cn, er * sn};
    const float nr = lam.r - 1.f, ni = lam.i, inv = 2.f / (lr * lr + li * li);
    s2 = cf{(nr * lr + ni * li) * inv, (ni * lr - nr * li) * inv};
    tag = ((u64)pe.x | ((u64)pe.y << 32)) * 0x9E3779B97F4A7C15ull + 0x632BE59BD9B4E019ull;
    tag |= 1ull;
  }

  Frag<NW> fa, fb;
  u32 sbase = 0;
  load(fa, 0, 0);
#pragma unroll 1
  for (int s = 0; s < 8; s++) {
    load(fb, sbase, 1);
    mma(fa); stats(fa, 0);
    load(fa, sbase, 2);
    mma(fb); stats(fb, 1);
    load(fb, sbase, 3);
    mma(fa); stats(fa, 2);
    stats(fb, 3);
    const u32 nbase = sbase == 2 * SLOT ? 0 : sbase + SLOT;
    if (s == 7) {
      const float a0 = half_sum(rs[0]), q0 = half_sum(rq[0]), a1 = half_sum(rs[1]), q1 = half_sum(rq[1]);
      const int mt = (ROLE == 3) ? (h ^ 1) : h;
      *(LAS f32x2*)(ROWP + ((mh * 64 + mt * 32 + l31) * 4 + ROLE) * 8) = h ? f32x2{a1, q1} : f32x2{a0, q0};
      const float c0 = half_sum(cs[0]), c1 = half_sum(cs[1]);
      constexpr int T0 = SSM ? 8 : J0;
      *(LAS float*)(COLP + ((mh * 10 + T0 + h) * 32 + l31) * 4) = h ? c1 : c0;
      if constexpr (EXTRA) {
        const float c2 = half_sum(cs[2]);
        *(LAS float*)(COLP + ((mh * 10 + 6 + (ROLE >> 1)) * 32 + l31) * 4) = c2;
      }
    }
    if (s < 6) wait_vm_bar<NG>(); else wait_vm_bar<0>();
    if (s < 7) load(fa, nbase, 0);
    if (s < 5) issue(s + 3, sbase);
    mma(fb);
    sbase = nbase;
  }
  STAMP(3);

  const __amdgpu_buffer_rsrc_t rws = __builtin_amdgcn_make_buffer_rsrc(p.ws, 0, WS_BYTES, 0x00020000);
  const int mt0 = rp * 4 + mh * 2;
  auto rowstat = [&](int row, float& m, float& r) {
    const f32x4 u = lds_rf4(ROWP + row * 32), v = lds_rf4(ROWP + row * 32 + 16);
    const float S = (u[0] + u[2]) + (v[0] + v[2]), Q = (u[1] + u[3]) + (v[1] + v[3]);
    m = S * (1.f / D);
    const float var = fmaxf(Q * (1.f / D) - m * m, 0.f);
    r = __builtin_amdgcn_rsqf(var + 1e-5f);
  };

  if constexpr (!SSM) {
    float m[2], r[2];
    rowstat(mh * 64 + (X0 >> 7) + l31, m[0], r[0]);
    rowstat(mh * 64 + (X1 >> 7) + l31, m[1], r[1]);
    if (ROLE == 2 && cg == 0) {
      const int row = rp * 128 + mh * 64 + h * 32 + tau(l31);
      const f32x2 v = h ? f32x2{m[1], r[1]} : f32x2{m[0], r[0]};
      __builtin_amdgcn_raw_buffer_store_b64(__builtin_bit_cast(v2u, v), rws, WS_STAT + row * 8, 0, 16);
    }
    auto ffn_tile = [&](const f32x16& a, const f32x4 (&c)[4], int mi, int j) {
      const float mm = m[mi], rr = r[mi];
      const float ke = rr * -1.4426950408889634f, r2 = rr * rr;
      float hv[8];
#pragma unroll
      for (int q = 0; q < 2; q++)
#pragma unroll
        for (int e = 0; e < 4; e++) {
          const float g = __builtin_fmaf(-mm, c[2 * q][e], a[8 * q + e]);
          const float u = __builtin_fmaf(-mm, c[2 * q + 1][e], a[8 * q + 4 + e]);
          const float ex = __builtin_amdgcn_exp2f(g * ke);
          const float rc = __builtin_amdgcn_rcpf(1.f + ex);
          hv[4 * q + e] = (g * u) * (r2 * rc);
        }
      v4u o = {pk2(hv[0], hv[1]), pk2(hv[2], hv[3]), pk2(hv[4], hv[5]), pk2(hv[6], hv[7])};
      const int mt = mt0 + ((mi ? X1 : X0) >> 12);
      __builtin_amdgcn_raw_buffer_store_b128(o, rws, WS_H + lane * 16 + (mt * 176 + cg * 11 + j) * 1024, 0, 16);
    };
#pragma unroll
    for (int t = 0; t < 2; t++) {
      f32x4 c[4];
#pragma unroll
      for (int q = 0; q < 4; q++) {
        const u32 a = COLP + ((J0 + t) * 32 + 8 * q + 4 * h) * 4;
        c[q] = lds_rf4(a) + lds_rf4(a + 1280);
      }
      ffn_tile(acc[0][t], c, 0, J0 + t);
      ffn_tile(acc[1][t], c, 1, J0 + t);
    }
    if constexpr (EXTRA) {
      f32x4 c[4];
#pragma unroll
      for (int q = 0; q < 4; q++) {
        const u32 a = COLP + (6 * 32 + 8 * q + 4 * h) * 4;
        c[q] = (lds_rf4(a) + lds_rf4(a + 1280)) + (lds_rf4(a + 128) + lds_rf4(a + 1280 + 128));
      }
      ffn_tile(accx, c, 0, 6);
    }
  } else {
    const u32 scr = SCR + mh * 9216;
    {
      float m, r;
      rowstat(mh * 64 + lane, m, r);
      const int ti = h * 32 + tau(l31);
      *(LAS float*)(scr + 8192 + ti * 4) = r;
      *(LAS float*)(scr + 8192 + 256 + ti * 4) = r * m;
    }
    const float cre = *(LAS float*)(COLP + (8 * 32 + l31) * 4) + *(LAS float*)(COLP + (18 * 32 + l31) * 4);
    const float cim = *(LAS float*)(COLP + (9 * 32 + l31) * 4) + *(LAS float*)(COLP + (19 * 32 + l31) * 4);
    cf E[2];
#pragma unroll
    for (int mi = 0; mi < 2; mi++) {
      cf e = {0.f, 0.f};
#pragma unroll
      for (int q = 0; q < 4; q++) {
        const f32x4 r4 = lds_rf4(scr + 8192 + (mi * 32 + h * 16 + q * 4) * 4);
        const f32x4 m4 = lds_rf4(scr + 8192 + 256 + (mi * 32 + h * 16 + q * 4) * 4);
#pragma unroll
        for (int k = 0; k < 4; k++) {
          const int i = q * 4 + k;
          const float ur = __builtin_fmaf(acc[mi][0][i], r4[k], -m4[k] * cre);
          const float ui = __builtin_fmaf(acc[mi][1][i], r4[k], -m4[k] * cim);
          acc[mi][0][i] = ur; acc[mi][1][i] = ui;
          e = cfma(lam, e, cf{ur, ui});
        }
      }
      E[mi] = e;
    }
    const cf l2 = cmul(lam, lam), l4 = cmul(l2, l2), l8 = cmul(l4, l4), l16 = cmul(l8, l8);
    const cf l32 = cmul(l16, l16), l64 = cmul(l32, l32), l128 = cmul(l64, l64);
    cf e0, e1, e2, e3;
    half_both(E[0].r, e0.r, e1.r); half_both(E[0].i, e0.i, e1.i);
    half_both(E[1].r, e2.r, e3.r); half_both(E[1].i, e2.i, e3.i);
    const cf z2 = cfma(l16, e0, e1), z3 = cfma(l16, z2, e2), G = cfma(l16, z3, e3);
    const int ch = rp * 2 + mh;
    const u32 tlo = (u32)tag, thi = (u32)(tag >> 32);
    if (h == 0) {
      const u32 gr = __float_as_uint(G.r), gi = __float_as_uint(G.i);
      __builtin_amdgcn_raw_buffer_store_b128(v4u{gr, tlo ^ gi, gi, thi ^ gr}, rws, WS_AGG + (cg * 32 + l31) * 16 + ch * 8192, 0, 16);
    }
#pragma unroll
    for (int mi = 0; mi < 2; mi++)
#pragma unroll
      for (int i = 0; i < 16; i++) {
        const cf v = cmul(s2, cf{acc[mi][0][i], acc[mi][1][i]});
        acc[mi][0][i] = v.r; acc[mi][1][i] = v.i;
      }
    cf S = {0.f, 0.f};
    if (ch > 0) {
      v4u g[16];
      const int j0 = ch - 1 - h;
      for (u32 spins = 0;; spins++) {
        bool ok = true;
#pragma unroll
        for (int q = 0; q < 16; q++) {
          const int j = j0 - 2 * q;
          const u32 off = j >= 0 ? (u32)(WS_AGG + (cg * 32 + l31) * 16 + j * 8192) : 0x80000000u;
          g[q] = __builtin_amdgcn_raw_buffer_load_b128(rws, off, 0, 16);
        }
#pragma unroll
        for (int q = 0; q < 16; q++) {
          const int j = j0 - 2 * q;
          ok = ok && (j < 0 || ((g[q].y ^ g[q].z) == tlo && (g[q].w ^ g[q].x) == thi));
        }
        if (__all(ok) || spins > (1u << 18)) break;
        __builtin_amdgcn_s_sleep(2);
      }
#pragma unroll
      for (int q = 15; q >= 0; q--) S = cfma(l128, S, cf{__uint_as_float(g[q].x), __uint_as_float(g[q].z)});
    }
    if (ch == 31 && lane == 0)
      __builtin_amdgcn_raw_buffer_store_b64(v2u{tlo, thi}, rws, WS_EPOCH + cg * 256, 0, 16);
    cf S0, S1;
    half_both(S.r, S0.r, S1.r); half_both(S.i, S0.i, S1.i);
    const cf C = cfma(l64, S1, S0);
    const cf l48 = cmul(l32, l16);
    const cf c0 = h ? cfma(l16, C, e0) : C;
    const cf c1 = h ? cfma(l48, C, z3) : cfma(l32, C, z2);
    cf hc[2] = {cmul(s2, c0), cmul(s2, c1)};
    const u32 wb = scr + l31 * 64;
    const u32 fsw = (l31 >> 1) & 3;
#pragma unroll
    for (int mi = 0; mi < 2; mi++) {
      u32 pr[8], pi[8];
      cf hh = hc[mi];
#pragma unroll
      for (int i = 0; i < 16; i += 2) {
        const cf a = cfma(lam, hh, cf{acc[mi][0][i], acc[mi][1][i]});
        hh = cfma(lam, a, cf{acc[mi][0][i + 1], acc[mi][1][i + 1]});
        pr[i >> 1] = pk2(a.r, hh.r);
        pi[i >> 1] = pk2(-a.i, -hh.i);
      }
#pragma unroll
      for (int q = 0; q < 2; q++) {
        const u32 co = ((2 * h + q) ^ fsw) << 4;
        *(LAS v4u*)(wb + mi * 4096 + co) = v4u{pr[4 * q], pr[4 * q + 1], pr[4 * q + 2], pr[4 * q + 3]};
        *(LAS v4u*)(wb + mi * 4096 + 2048 + co) = v4u{pi[4 * q], pi[4 * q + 1], pi[4 * q + 2], pi[4 * q + 3]};
      }
    }
    wait_lgkm();
    {
      const int g1 = (lane >> 4) & 1, a = (lane & 15) >> 2, pp = lane & 3;
      const int cki = 2 * (pp & 1) + g1;
      const u32 r0 = scr + (8 * h + a) * 64 + ((cki ^ (a >> 1)) << 4) + 8 * (pp >> 1);
      const u32 r1 = scr + (8 * h + 4 + a) * 64 + ((cki ^ (2 + (a >> 1))) << 4) + 8 * (pp >> 1);
#pragma unroll
      for (int mi = 0; mi < 2; mi++)
#pragma unroll
        for (int q = 0; q < 4; q++) {
          const u32 o = mi * 4096 + q * 1024;
          const bf16x8 v = lds_tr2(r0 + o, r1 + o);
          __builtin_amdgcn_raw_buffer_store_b128(__builtin_bit_cast(v4u, v), rws,
                                                 WS_H + lane * 16 + ((mt0 + mi) * 176 + cg * 11 + 7 + q) * 1024, 0, 16);
        }
    }
  }
  STAMP(4);
  STAMP_FLUSH(0, 5);
}

__global__ __launch_bounds__(512, 2) void k1(Params p) {
  const int b = blockIdx.x, xcd = b & 7, i = b >> 3;
  const int rp = (xcd & 1) * 8 + (i & 7), cg = (xcd >> 1) * 4 + (i >> 3);
  const int w = __builtin_amdgcn_readfirstlane(threadIdx.x >> 6), lane = threadIdx.x & 63;
  STAMP_DECL;
  STAMP(0);
  const int role = (w & 1) * 2 + (w >> 2);
  if (role == 0) phase1<0>(p, w, lane, rp, cg STAMP_PASS);
  else if (role == 1) phase1<1>(p, w, lane, rp, cg STAMP_PASS);
  else if (role == 2) phase1<2>(p, w, lane, rp, cg STAMP_PASS);
  else phase1<3>(p, w, lane, rp, cg STAMP_PASS);
}

__global__ __launch_bounds__(512, 2) void k2(Params p) {
  const int b = blockIdx.x, xcd = b & 7, i = b >> 3;
  const int rt = xcd * 4 + (i >> 3), ct = i & 7;
  const int w = __builtin_amdgcn_readfirstlane(threadIdx.x >> 6), lane = threadIdx.x & 63;
  const int l31 = lane & 31, h = lane >> 5;
  STAMP_DECL;
  STAMP(8);
  const v4u s2 = make_srd(p.w2, F * D * 2), scr = make_srd(p.cre, D * P * 2), sci = make_srd(p.cim, D * P * 2);
  const __amdgpu_buffer_rsrc_t rws = __builtin_amdgcn_make_buffer_rsrc(p.ws, 0, WS_BYTES, 0x00020000);
  const u32 WB = w * 20480;
  const int r8 = lane >> 3, c8 = lane & 7;
  const u32 vf = (u32)((224 * w + r8) * 1024 + ct * 128 + ((c8 ^ (((r8 >> 1) & 1) << 2)) << 4));
  const u32 vc0 = (u32)((64 * ct + r8) * 1024 + w * 128 + ((c8 ^ (r8 >> 1)) << 4)), vc1 = vc0 ^ 64u;
#pragma unroll
  for (int pi = 0; pi < 20; pi++) glds16(WB + pi * 1024, vf, s2, pi * 8192);

  const int g1 = (lane >> 4) & 1, a = (lane & 15) >> 2, pp = lane & 3;
  const u32 fa0 = WB + (4 * h + a) * 128 + ((4 * (a >> 1) + 2 * g1 + (pp >> 1)) << 4) + 8 * (pp & 1), fa1 = fa0 ^ 64u;
  bf16x8 Bf[12][2];
  wait_vm<0>();
#pragma unroll
  for (int q = 0; q < 10; q++) {
    Bf[q][0] = lds_tr2(fa0 + q * 2048, fa0 + q * 2048 + 1024);
    Bf[q][1] = lds_tr2(fa1 + q * 2048, fa1 + q * 2048 + 1024);
  }
  wait_lgkm();
#pragma unroll
  for (int pi = 0; pi < 4; pi++) glds16(WB + pi * 1024, vf, s2, (20 + pi) * 8192);
#pragma unroll
  for (int pi = 0; pi < 8; pi++) glds16(WB + 4096 + pi * 1024, (pi & 1) ? vc1 : vc0, scr, pi * 8192);
#pragma unroll
  for (int pi = 0; pi < 8; pi++) glds16(WB + 12288 + pi * 1024, (pi & 1) ? vc1 : vc0, sci, pi * 8192);
  wait_vm<16>();
#pragma unroll
  for (int q = 0; q < 2; q++) {
    Bf[10 + q][0] = lds_tr2(fa0 + q * 2048, fa0 + q * 2048 + 1024);
    Bf[10 + q][1] = lds_tr2(fa1 + q * 2048, fa1 + q * 2048 + 1024);
  }
  wait_lgkm();
#pragma unroll
  for (int pi = 0; pi < 4; pi++) glds16(WB + pi * 1024, vf, s2, (24 + pi) * 8192);
  STAMP(9);

  f32x16 acc[2][2];
#pragma unroll
  for (int k = 0; k < 16; k++) { acc[0][0][k] = 0.f; acc[0][1][k] = 0.f; acc[1][0][k] = 0.f; acc[1][1][k] = 0.f; }
  const u32 hb = WS_H + lane * 16;
  const u32 hs0 = (u32)((2 * rt * 176 + 22 * w) * 1024), hs1 = hs0 + 176 * 1024;
  auto lda = [&](int kb, int mi) {
    return __builtin_bit_cast(bf16x8, __builtin_amdgcn_raw_buffer_load_b128(rws, hb + kb * 1024, mi ? hs1 : hs0, 16));
  };
  constexpr int NST = 22, WIN = 8;
  auto kb_of = [](int st) {
    if (st < 14) return (st / 7) * 11 + (st % 7);
    const int u = st - 14, mat = u >> 2, kb4 = u & 3;
    return (kb4 >> 1) * 11 + 7 + 2 * mat + (kb4 & 1);
  };
  bf16x8 Af[WIN][2];
#pragma unroll
  for (int st = 0; st < WIN; st++) { Af[st][0] = lda(kb_of(st), 0); Af[st][1] = lda(kb_of(st), 1); }
  __builtin_amdgcn_sched_barrier(0);
  const u32 ca = WB + 4096 + l31 * 128;
  const u32 csw = (u32)((h ^ ((l31 >> 1) & 7)) << 4);
#pragma unroll
  for (int st = 0; st < NST; st++) {
    bf16x8 b0, b1;
    if (st < 12) { b0 = Bf[st][0]; b1 = Bf[st][1]; }
    else if (st < 14) {
      b0 = lds_tr2(fa0 + (st - 12) * 2048, fa0 + (st - 12) * 2048 + 1024);
      b1 = lds_tr2(fa1 + (st - 12) * 2048, fa1 + (st - 12) * 2048 + 1024);
    } else {
      const int u = st - 14, mat = u >> 2, kb4 = u & 3;
      const u32 ad = ca + (csw ^ (u32)(kb4 << 5)) + mat * 8192;
      b0 = lds_r128(ad);
      b1 = lds_r128(ad + 4096);
    }
    const bf16x8 a0 = Af[st % WIN][0], a1 = Af[st % WIN][1];
    acc[0][0] = mfma(b0, a0, acc[0][0]);
    acc[0][1] = mfma(b1, a0, acc[0][1]);
    acc[1][0] = mfma(b0, a1, acc[1][0]);
    acc[1][1] = mfma(b1, a1, acc[1][1]);
    if (st + WIN < NST) { Af[st % WIN][0] = lda(kb_of(st + WIN), 0); Af[st % WIN][1] = lda(kb_of(st + WIN), 1); }
    __builtin_amdgcn_sched_barrier(0);
  }
  STAMP(10);
  const int tid = threadIdx.x;
  v2u xv[2]; f32x2 st2[2];
  const int dq = tid & 15;
  const v2u dk = *(const v2u*)(p.dsk + ct * 64 + dq * 4);
  int grow[2];
#pragma unroll
  for (int e = 0; e < 2; e++) {
    const int rr = (tid >> 4) + 32 * e;
    grow[e] = rt * 64 + (rr & 32) + tau(rr & 31);
    xv[e] = *(const v2u*)(p.x + grow[e] * D + ct * 64 + dq * 4);
    st2[e] = __builtin_bit_cast(f32x2, __builtin_amdgcn_raw_buffer_load_b64(rws, WS_STAT + grow[e] * 8, 0, 16));
  }
  asm volatile("s_waitcnt lgkmcnt(0)\n\ts_barrier" ::: "memory");
#pragma unroll
  for (int mi = 0; mi < 2; mi++)
#pragma unroll
    for (int c = 0; c < 2; c++)
#pragma unroll
      for (int j = 0; j < 4; j++) {
        const u32 pos = (u32)(lane ^ (c * 8 + j * 2 + h));
        *(LAS f32x4*)(w * 16384 + (((mi * 2 + c) * 4 + j) * 64 + pos) * 16) =
            f32x4{acc[mi][c][4 * j], acc[mi][c][4 * j + 1], acc[mi][c][4 * j + 2], acc[mi][c][4 * j + 3]};
      }
  asm volatile("s_waitcnt lgkmcnt(0)\n\ts_barrier" ::: "memory");
#pragma unroll
  for (int e = 0; e < 2; e++) {
    const int rho = (tid >> 4) & 31;
    const u32 ad = (u32)((((e * 2 + (dq >> 3)) * 4 + ((dq >> 1) & 3)) * 64 + ((rho + 32 * (dq & 1)) ^ dq)) * 16);
    f32x4 s = lds_rf4(ad);
#pragma unroll
    for (int k = 1; k < 8; k++) s += lds_rf4(ad + k * 16384);
    const float m = st2[e][0], r = st2[e][1];
    const float x0 = bf2f(xv[e].x & 0xffffu), x1 = bf2f(xv[e].x >> 16), x2 = bf2f(xv[e].y & 0xffffu), x3 = bf2f(xv[e].y >> 16);
    const float d0 = bf2f(dk.x & 0xffffu) * r, d1 = bf2f(dk.x >> 16) * r, d2 = bf2f(dk.y & 0xffffu) * r, d3 = bf2f(dk.y >> 16) * r;
    f32x4 o;
    o[0] = s[0] + x0 + (x0 - m) * d0;
    o[1] = s[1] + x1 + (x1 - m) * d1;
    o[2] = s[2] + x2 + (x2 - m) * d2;
    o[3] = s[3] + x3 + (x3 - m) * d3;
    *(f32x4*)(p.out + grow[e] * D + ct * 64 + dq * 4) = o;
  }
  STAMP(11);
  STAMP_FLUSH(8, 12);
}

extern "C" void kernel_launch(void* const* d_in, const int* in_sizes, int n_in, void* d_out, int out_size, void* d_ws,
                              size_t ws_size, hipStream_t stream) {
  Params p;
  p.x = (const u16*)d_in[0];
  p.lre = (const u16*)d_in[1];
  p.lim = (const u16*)d_in[2];
  p.bre = (const u16*)d_in[3];
  p.bim = (const u16*)d_in[4];
  p.cre = (const u16*)d_in[5];
  p.cim = (const u16*)d_in[6];
  p.ldt = (const u16*)d_in[7];
  p.dsk = (const u16*)d_in[8];
  p.w1 = (const u16*)d_in[10];
  p.w3 = (const u16*)d_in[11];
  p.w2 = (const u16*)d_in[12];
  p.out = (float*)d_out;
  p.ws = (char*)d_ws;
  hipFuncSetAttribute((const void*)k1, hipFuncAttributeMaxDynamicSharedMemorySize, LDS_BYTES);
  hipFuncSetAttribute((const void*)k2, hipFuncAttributeMaxDynamicSharedMemorySize, LDS_BYTES);
  k1<<<256, 512, LDS_BYTES, stream>>>(p);
  k2<<<256, 512, LDS_BYTES, stream>>>(p);
}
```

```cpp
#include <hip/hip_runtime.h>
#include <stdint.h>

typedef unsigned short u16;
typedef unsigned int u32;
typedef unsigned long long u64;
typedef __attribute__((ext_vector_type(8))) __bf16 bf16x8;
typedef __attribute__((ext_vector_type(4))) __bf16 bf16x4;
typedef __attribute__((ext_vector_type(2))) __bf16 bf16x2;
typedef __attribute__((ext_vector_type(16))) float f32x16;
typedef __attribute__((ext_vector_type(4))) float f32x4;
typedef __attribute__((ext_vector_type(2))) float f32x2;
typedef __attribute__((ext_vector_type(4))) u32 v4u;
typedef __attribute__((ext_vector_type(2))) u32 v2u;
#define LAS __attribute__((address_space(3)))
#define DEVI __device__ __forceinline__
#define SBAR() __builtin_amdgcn_sched_barrier(0)

constexpr int L = 2048, D = 512, P = 512, F = 1792;

constexpr u32 WS_EPOCH = 0;
constexpr u32 WS_RS = 4096;
constexpr u32 WS_CS = WS_RS + 2048 * 16;
constexpr u32 WS_AGG = WS_CS + 16 * 8 * 144 * 16;
constexpr u32 WS_FLAG = WS_AGG + 32 * 512 * 16;
constexpr u32 WS_STAMP = 1u << 20;
constexpr u32 WS_H = 2u << 20;
constexpr u32 H_BYTES = 64u * 176u * 1024u;
constexpr u32 WS_BYTES = WS_H + H_BYTES;
constexpr u32 OOB = 0x10000000u;

constexpr u32 XO = 0, BSO = 16384, W1O = 24576, W3O = 24576 + 14336 + 128, SLOT = W3O + 14336;
constexpr u32 TAIL = 49152;
constexpr u32 WC = 2 * SLOT + TAIL, XC = WC + 4608, RT_R = XC + 2048, POW = RT_R + 512, ZERO = POW + 512, LFLAG = ZERO + 16;
static_assert(POW >= 3 * SLOT && XC >= 3 * SLOT && LFLAG + 32 <= 163840, "lds map");
constexpr u32 LDS_BYTES = 163840;
constexpr u32 SPIN_CAP = 1u << 17;
#define ROT 2

struct Params {
  const u16 *x, *lre, *lim, *bre, *bim, *cre, *cim, *ldt, *dsk, *w1, *w3, *w2;
  float* out;
  char* ws;
};

DEVI v4u make_srd(const void* p, u32 bytes) {
  u64 a = (u64)p;
  return v4u{(u32)a, (u32)(a >> 32) & 0xffffu, bytes, 0x00020000u};
}
DEVI void glds16(u32 lds, u32 voff, v4u srd, u32 soff) {
  lds = __builtin_amdgcn_readfirstlane(lds);
  asm volatile("s_mov_b32 m0, %0\n\ts_nop 0\n\tbuffer_load_dwordx4 %1, %2, %3 offen lds" ::"s"(lds), "v"(voff), "s"(srd), "s"(soff)
               : "memory");
}
DEVI void glds4(u32 lds, u32 voff, v4u srd) {
  asm volatile("s_mov_b32 m0, %0\n\ts_nop 0\n\tbuffer_load_dword %1, %2, 0 offen lds" ::"s"(lds), "v"(voff), "s"(srd) : "memory");
}
template <int N> DEVI void wait_vm_bar() {
  asm volatile("s_waitcnt vmcnt(%0) lgkmcnt(0)\n\ts_barrier" ::"n"(N) : "memory");
}
DEVI void lds_bar() { asm volatile("s_waitcnt lgkmcnt(0)\n\ts_barrier" ::: "memory"); }
template <int N> DEVI void wait_vm() { asm volatile("s_waitcnt vmcnt(%0)" ::"n"(N) : "memory"); }
DEVI void wait_lgkm() { asm volatile("s_waitcnt lgkmcnt(0)" ::: "memory"); }
DEVI float bf2f(u32 v) { return __uint_as_float(v << 16); }
DEVI float bfhi(u32 v) { return __uint_as_float(v & 0xffff0000u); }
template <class T> DEVI T lds_r(u32 a) { return *(LAS T*)(uintptr_t)a; }
template <class T> DEVI void lds_w(u32 a, T v) { *(LAS T*)(uintptr_t)a = v; }
DEVI bf16x8 lds_tr2(u32 a0, u32 a1) {
  bf16x4 lo = __builtin_amdgcn_ds_read_tr16_b64_v4bf16((LAS bf16x4*)(uintptr_t)a0);
  bf16x4 hi = __builtin_amdgcn_ds_read_tr16_b64_v4bf16((LAS bf16x4*)(uintptr_t)a1);
  return __builtin_shufflevector(lo, hi, 0, 1, 2, 3, 4, 5, 6, 7);
}
DEVI f32x16 mfma(bf16x8 a, bf16x8 b, f32x16 c) { return __builtin_amdgcn_mfma_f32_32x32x16_bf16(a, b, c, 0, 0, 0); }
DEVI float dot_ones(bf16x8 a, float c) {
  const bf16x2 o = {(__bf16)1.0f, (__bf16)1.0f};
  c = __builtin_amdgcn_fdot2_f32_bf16(__builtin_shufflevector(a, a, 0, 1), o, c, false);
  c = __builtin_amdgcn_fdot2_f32_bf16(__builtin_shufflevector(a, a, 2, 3), o, c, false);
  c = __builtin_amdgcn_fdot2_f32_bf16(__builtin_shufflevector(a, a, 4, 5), o, c, false);
  c = __builtin_amdgcn_fdot2_f32_bf16(__builtin_shufflevector(a, a, 6, 7), o, c, false);
  return c;
}
template <int CTRL> DEVI float dppx(float v) {
  return __int_as_float(__builtin_amdgcn_update_dpp(0, __float_as_int(v), CTRL, 0xf, 0xf, true));
}
DEVI float half_sum(float v) {
  auto r = __builtin_amdgcn_permlane32_swap(__float_as_uint(v), __float_as_uint(v), false, false);
  return __uint_as_float(r[0]) + __uint_as_float(r[1]);
}
DEVI float wave_sum(float v) {
  v += dppx<0xB1>(v);
  v += dppx<0x4E>(v);
  v += dppx<0x141>(v);
  v += dppx<0x140>(v);
  auto r = __builtin_amdgcn_permlane16_swap(__float_as_uint(v), __float_as_uint(v), false, false);
  return half_sum(__uint_as_float(r[0]) + __uint_as_float(r[1]));
}
DEVI u32 pk2(float a, float b) {
  bf16x2 v = {(__bf16)a, (__bf16)b};
  return __builtin_bit_cast(u32, v);
}
DEVI int tau(int r) { return 16 * ((r >> 2) & 1) + 4 * (r >> 3) + (r & 3); }
DEVI f32x2 fma2(f32x2 a, f32x2 b, f32x2 c) { return __builtin_elementwise_fma(a, b, c); }
DEVI f32x2 dup(float v) { f32x2 r = {v, v}; asm("" : "+v"(r)); return r; }
DEVI float smul(float a, float b) { float r; asm("v_mul_f32 %0, %1, %2" : "=v"(r) : "v"(a), "v"(b)); return r; }
DEVI float sfma(float a, float b, float c) { float r; asm("v_fma_f32 %0, %1, %2, %3" : "=v"(r) : "v"(a), "v"(b), "v"(c)); return r; }
DEVI float sfms(float a, float b, float c) { float r; asm("v_fma_f32 %0, -%1, %2, %3" : "=v"(r) : "v"(a), "v"(b), "v"(c)); return r; }
struct cf { float r, i; };
DEVI cf cfma(cf a, cf b, cf c) { return cf{sfma(a.r, b.r, sfms(a.i, b.i, c.r)), sfma(a.r, b.i, sfma(a.i, b.r, c.i))}; }
DEVI cf cmul(cf a, cf b) { return cf{sfms(a.i, b.i, smul(a.r, b.r)), sfma(a.r, b.i, smul(a.i, b.r))}; }
DEVI void half_both(cf v, cf& lo, cf& hi) {
  asm volatile("s_nop 1" : "+v"(v.r), "+v"(v.i));
  auto r = __builtin_amdgcn_permlane32_swap(__float_as_uint(v.r), __float_as_uint(v.r), false, false);
  auto q = __builtin_amdgcn_permlane32_swap(__float_as_uint(v.i), __float_as_uint(v.i), false, false);
  lo = cf{__uint_as_float(r[0]), __uint_as_float(q[0])};
  hi = cf{__uint_as_float(r[1]), __uint_as_float(q[1])};
}
DEVI v4u mkrec(float a, float b, u32 tlo, u32 thi) {
  const u32 x = __float_as_uint(a), y = __float_as_uint(b);
  return v4u{x, tlo ^ y, y, thi ^ x};
}
DEVI bool recok(v4u g, u32 tlo, u32 thi) { return (g.y ^ g.z) == tlo && (g.w ^ g.x) == thi; }

#define STAMP(i) do {} while (0)
#define DSTAMP(i) do {} while (0)
#define STAMP_DECL
#define STAMP_ARG
#define STAMP_PASS
#define STAMP_FLUSH(lo, hi) do {} while (0)

DEVI u32 upos(int i, int w) { return (u32)(((i / 3 + 2) % 3) * SLOT + (i % 3) * 2048) + (u32)w * 6144u; }

DEVI u32 scan_scr(int mh) { return (mh ? 0u : SLOT) + TAIL; }
DEVI void lds_spin(u32 a) {
#pragma unroll 1
  for (u32 spins = 0; spins < (SPIN_CAP << 6); spins++) {
    if (__builtin_amdgcn_readfirstlane(*(volatile LAS u32*)(uintptr_t)a) != 0u) break;
    __builtin_amdgcn_s_sleep(1);
  }
  asm volatile("" ::: "memory");
}

DEVI void glds16x4(u32 lds, u32 v0, u32 v1, u32 v2, u32 v3, v4u srd, u32 soff) {
  lds = __builtin_amdgcn_readfirstlane(lds);
  asm volatile("s_mov_b32 m0, %0\n\ts_nop 0\n\tbuffer_load_dwordx4 %1, %5, %6 offen lds\n\t"
               "buffer_load_dwordx4 %2, %5, %6 offen offset:1024 lds\n\t"
               "buffer_load_dwordx4 %3, %5, %6 offen offset:2048 lds\n\t"
               "buffer_load_dwordx4 %4, %5, %6 offen offset:3072 lds" ::"s"(lds), "v"(v0), "v"(v1), "v"(v2), "v"(v3), "s"(srd), "s"(soff)
               : "memory");
}
DEVI void glds16x3(u32 lds, u32 v0, u32 v1, u32 v2, v4u srd, u32 soff) {
  lds = __builtin_amdgcn_readfirstlane(lds);
  asm volatile("s_mov_b32 m0, %0\n\ts_nop 0\n\tbuffer_load_dwordx4 %1, %4, %5 offen lds\n\t"
               "buffer_load_dwordx4 %2, %4, %5 offen offset:1024 lds\n\t"
               "buffer_load_dwordx4 %3, %4, %5 offen offset:2048 lds" ::"s"(lds), "v"(v0), "v"(v1), "v"(v2), "s"(srd), "s"(soff)
               : "memory");
}
DEVI void glds16x2(u32 lds, u32 v0, u32 v1, v4u srd, u32 soff) {
  lds = __builtin_amdgcn_readfirstlane(lds);
  asm volatile("s_mov_b32 m0, %0\n\ts_nop 0\n\tbuffer_load_dwordx4 %1, %3, %4 offen lds\n\t"
               "buffer_load_dwordx4 %2, %3, %4 offen offset:1024 lds" ::"s"(lds), "v"(v0), "v"(v1), "s"(srd), "s"(soff)
               : "memory");
}
DEVI void split3(float v, u32& a, u32& b, u32& c) {
  const __bf16 x = (__bf16)v;
  const float r1 = v - (float)x;
  const __bf16 y = (__bf16)r1;
  const __bf16 z = (__bf16)(r1 - (float)y);
  a = __builtin_bit_cast(u16, x); b = __builtin_bit_cast(u16, y); c = __builtin_bit_cast(u16, z);
}

struct Ctx {
  int w, lane, rp, cg, ct;
  u32 tlo, thi;
};

DEVI u32 frag_xa(int lane, int ks) { return (u32)((lane & 31) * 128 + (((2 * ks + (lane >> 5)) ^ (((lane & 31) >> 1) & 7)) << 4)); }
DEVI u32 frag_wa(int lane) {
  const int g1 = (lane >> 4) & 1, a = (lane & 15) >> 2, pp = lane & 3;
  return ((pp >> 1) ? W3O : W1O) + (8 * (lane >> 5) + a) * 224 + (8 * g1 + 4 * (pp & 1)) * 2;
}
DEVI void make_tag(Ctx& c, v2u pe) {
  const u64 tag = (((u64)pe.x | ((u64)pe.y << 32)) * 0x9E3779B97F4A7C15ull + 0x632BE59BD9B4E019ull) | 1ull;
  c.tlo = __builtin_amdgcn_readfirstlane((u32)tag);
  c.thi = __builtin_amdgcn_readfirstlane((u32)(tag >> 32));
}

DEVI void loader(const Params& p, Ctx& c STAMP_ARG) {
  const int lw = c.w - 4, lane = c.lane, rp = c.rp, cg = c.cg;
  const int l31 = lane & 31, r8 = lane >> 3, c8 = lane & 7;
  constexpr int RM = 8 / ROT;
  const int sig = RM * (((rp / RM) + cg) & (ROT - 1));
  const int cst = rp & (RM - 1);
  const v4u sx = make_srd(p.x, L * D * 2);
  const v4u sb = make_srd((lw >> 1) ? p.bim : p.bre, P * D * 2);
  const v4u sw13 = make_srd((lw >> 1) ? p.w3 : p.w1, D * F * 2);
  const v4u s2w = make_srd(p.w2, F * D * 2);
  const v4u sws = make_srd(p.ws, WS_BYTES);
  const __amdgpu_buffer_rsrc_t rws = __builtin_amdgcn_make_buffer_rsrc(p.ws, 0, WS_BYTES, 0x00020000);

  u32 vx[4], vb[2], vw[7];
#pragma unroll
  for (int e = 0; e < 4; e++)
    vx[e] = (u32)((rp * 128 + lw * 32 + tau(8 * e + r8)) * 1024 + ((c8 ^ ((4 * e + (r8 >> 1)) & 7)) << 4) - e * 1024);
#pragma unroll
  for (int e = 0; e < 2; e++)
    vb[e] = (u32)((cg * 32 + (lw & 1) * 16 + 8 * e + r8) * 1024 + ((c8 ^ ((4 * e + (r8 >> 1)) & 7)) << 4) - e * 1024);
#pragma unroll
  for (int i = 0; i < 7; i++) {
    const int ci = i * 64 + lane;
    const int kr = (ci * 4682) >> 16, cj = ci - kr * 14;
    vw[i] = (u32)(((lw & 1) * 32 + kr) * (F * 2) + cj * 16 + cg * 224 - (i & 3) * 1024);
  }
  const u32 wd = ((lw >> 1) ? W3O : W1O) + (lw & 1) * 7168;
  auto issue = [&](int g, u32 sbase) {
    const u32 st = (u32)((g + sig) & 7);
    const u32 so = st * 128, sw = st * (64 * F * 2);
    glds16x4(sbase + XO + lw * 4096, vx[0], vx[1], vx[2], vx[3], sx, so);
    glds16x4(sbase + wd, vw[0], vw[1], vw[2], vw[3], sw13, sw);
    glds16x3(sbase + wd + 4096, vw[4], vw[5], vw[6], sw13, sw);
    glds16x2(sbase + BSO + lw * 2048, vb[0], vb[1], sb, so);
  };
  issue(0, 0);
  v2u pe;
  asm volatile("buffer_load_dwordx2 %0, off, %1, 0 sc1" : "=&v"(pe) : "s"(sws) : "memory");
  STAMP(1);
  issue(1, SLOT);
  if (lane < 12) lds_w<u32>(ZERO + lane * 4, 0u);
  wait_vm_bar<13>();
  STAMP(2);
  issue(2, 2 * SLOT);
  {
    const int b = blockIdx.x;
    const char* base = lw < 2 ? (const char*)p.w2 : lw == 2 ? (const char*)p.cre : (const char*)p.cim;
    const u32 nb = lw < 2 ? (u32)(F * D * 2) : (u32)(D * P * 2);
    u32 off;
    if (lw < 2) off = lw * 64 + lane < 112 ? (u32)(b * 7168 + (lw * 64 + lane) * 64) : OOB;
    else off = lane < 32 ? (u32)(b * 2048 + lane * 64) : OOB;
    glds4(XC, off, make_srd(base, nb));
  }
  float pva = 0.f, pvb = 0.f;
  u32 poff = OOB;
  const u32 wa = frag_wa(lane);
  auto colsum = [&](u32 sbase, int stage) {
    const int tile = (rp >> 3) * 4 + lw;
    float cs = 0.f, c8s = 0.f;
    if (tile < 7) {
#pragma unroll
      for (int ks = 0; ks < 4; ks++) cs = dot_ones(lds_tr2(sbase + wa + tile * 32 + ks * 3584, sbase + wa + tile * 32 + ks * 3584 + 896), cs);
    } else {
#pragma unroll
      for (int ks = 0; ks < 4; ks++) {
        cs = dot_ones(lds_r<bf16x8>(sbase + BSO + frag_xa(lane, ks)), cs);
        c8s = dot_ones(lds_r<bf16x8>(sbase + BSO + 4096 + frag_xa(lane, ks)), c8s);
      }
    }
    cs = half_sum(cs);
    c8s = half_sum(c8s);
    const float n1 = dppx<0xB1>(cs), n8 = dppx<0xB1>(c8s);
    if (lane < 32) {
      pva = (lane & 1) ? n8 : cs;
      pvb = (lane & 1) ? c8s : n1;
      poff = (lane & 1) ? (tile == 7 ? WS_CS + ((cg * 8 + stage) * 144 + 128 + (lane >> 1)) * 16 : OOB)
                        : WS_CS + ((cg * 8 + stage) * 144 + tile * 16 + (lane >> 1)) * 16;
    }
  };
  if (cst == 0) colsum(0, sig);
  wait_vm_bar<14>();
  asm volatile("" : "+v"(pe));
  make_tag(c, pe);
  const u32 tlo = c.tlo, thi = c.thi;
  const int lt = lw * 64 + lane;
  const u32 gco = lt < 144 ? WS_CS + (cg * 8 * 144 + lt) * 16 : OOB;
  const u32 gro = lt >= 128 ? WS_RS + (rp * 128 + lt - 128) * 16 : OOB;
  v4u gr[9];
  u32 sbase = SLOT, pbase = 0;
#pragma unroll 1
  for (int s = 1; s < 5; s++) {
    __builtin_amdgcn_raw_buffer_store_b128(mkrec(pva, pvb, tlo, thi), rws, poff, 0, 16);
    poff = OOB;
    issue(s + 2, pbase);
    if (cst == s) colsum(sbase, (s + sig) & 7);
    wait_vm_bar<13>();
    pbase = sbase;
    sbase = sbase == 2 * SLOT ? 0 : sbase + SLOT;
  }
  issue(7, SLOT);
  wait_vm_bar<13>();
  const u32 vf = (u32)((224 * lw + r8) * 1024 + c.ct * 128 + ((c8 ^ (((r8 >> 1) & 1) << 2)) << 4));
  const u32 vf1 = vf + 7168, vf2 = vf + 14336, vf3 = vf + 21504;
  auto issue_v = [&](int g, u32 sb_) {
#pragma unroll
    for (int k = 0; k < 2; k++) {
      const u32 so = (u32)(g * 49152 + k * (224 * 4 * 1024));
      glds16x4(sb_ + (lw + 4 * k) * 6144, vf, vf1, vf2, vf3, s2w, so);
      glds16x2(sb_ + (lw + 4 * k) * 6144 + 4096, vf, vf1, s2w, so + 32768);
    }
  };
  asm volatile("buffer_load_dwordx4 %0, %9, %11, 0 offen sc1\n\tbuffer_load_dwordx4 %1, %9, %11, 0 offen offset:2304 sc1\n\t"
               "buffer_load_dwordx4 %2, %9, %11, %12 offen sc1\n\tbuffer_load_dwordx4 %3, %9, %11, %12 offen offset:2304 sc1\n\t"
               "buffer_load_dwordx4 %4, %9, %11, %13 offen sc1\n\tbuffer_load_dwordx4 %5, %9, %11, %13 offen offset:2304 sc1\n\t"
               "buffer_load_dwordx4 %6, %9, %11, %14 offen sc1\n\tbuffer_load_dwordx4 %7, %9, %11, %14 offen offset:2304 sc1\n\t"
               "buffer_load_dwordx4 %8, %10, %11, 0 offen sc1"
               : "=&v"(gr[0]), "=&v"(gr[1]), "=&v"(gr[2]), "=&v"(gr[3]), "=&v"(gr[4]), "=&v"(gr[5]), "=&v"(gr[6]), "=&v"(gr[7]), "=&v"(gr[8])
               : "v"(gco), "v"(gro), "s"(sws), "s"(4608u), "s"(9216u), "s"(13824u)
               : "memory");
  issue_v(0, 2 * SLOT);
  wait_vm_bar<12>();
  issue_v(1, 0);
  wait_vm<24>();
  asm volatile("" : "+v"(gr[0]), "+v"(gr[1]), "+v"(gr[2]), "+v"(gr[3]), "+v"(gr[4]), "+v"(gr[5]), "+v"(gr[6]), "+v"(gr[7]), "+v"(gr[8]));
  {
    bool ok = lt < 128 || recok(gr[8], tlo, thi);
#pragma unroll
    for (int k = 0; k < 8; k++) ok = ok & (lt >= 144 || recok(gr[k], tlo, thi));
    if (!__all(ok)) {
#pragma unroll 1
      for (u32 spins = 0; spins < SPIN_CAP; spins++) {
#pragma unroll
        for (int k = 0; k < 8; k++) gr[k] = __builtin_amdgcn_raw_buffer_load_b128(rws, gco + k * 2304, 0, 16);
        gr[8] = __builtin_amdgcn_raw_buffer_load_b128(rws, gro, 0, 16);
        ok = lt < 128 || recok(gr[8], tlo, thi);
#pragma unroll
        for (int k = 0; k < 8; k++) ok = ok & (lt >= 144 || recok(gr[k], tlo, thi));
        if (__all(ok)) break;
        __builtin_amdgcn_s_sleep(2);
      }
    }
    if (lt < 144) {
      float a = 0.f, b = 0.f;
#pragma unroll
      for (int k = 0; k < 8; k++) { a += __uint_as_float(gr[k].x); b += __uint_as_float(gr[k].z); }
      u32 h0, m0, l0, h1, m1, l1;
      split3(a, h0, m0, l0);
      split3(b, h1, m1, l1);
      const u32 dst = WC + lt * 32;
      lds_w<v4u>(dst, v4u{h0 | (h0 << 16), m0 | (m0 << 16), h0 | (l0 << 16), 0u});
      lds_w<v4u>(dst + 16, v4u{h1 | (h1 << 16), m1 | (m1 << 16), h1 | (l1 << 16), 0u});
    }
    if (lt >= 128) {
      const int ti = lt - 128;
      const int rho = (ti & 3) | ((ti >> 2) & 4) | ((ti << 1) & 24);
      u32 hh, mm, ll;
      split3(-__uint_as_float(gr[8].x), hh, mm, ll);
      lds_w<v4u>(XC + ((ti & 96) + rho) * 16, v4u{hh | (mm << 16), hh | (mm << 16), ll | (hh << 16), 0u});
      lds_w<u32>(RT_R + ti * 4, gr[8].z);
    }
  }
  lds_bar();
  STAMP(3);
}

DEVI void carry_job(const Params& p, const Ctx& c, int mh) {
  const __amdgpu_buffer_rsrc_t rws = __builtin_amdgcn_make_buffer_rsrc(p.ws, 0, WS_BYTES, 0x00020000);
  const int l31 = c.lane & 31, h = c.lane >> 5;
  const int ch = c.rp * 2 + mh;
  const f32x4 pw = lds_r<f32x4>(POW + l31 * 16);
  const cf l64 = {pw[0], pw[1]}, l128 = {pw[2], pw[3]};
  cf S = {0.f, 0.f};
  if (ch > 0) {
    v4u g[16];
    const int j0 = ch - 1 - h;
#pragma unroll 1
    for (u32 spins = 0;; spins++) {
      bool ok = true;
#pragma unroll
      for (int q = 0; q < 16; q++) {
        const int j = j0 - 2 * q;
        const u32 off = j >= 0 ? (u32)(WS_AGG + (c.cg * 32 + l31) * 16 + j * 8192) : OOB;
        g[q] = __builtin_amdgcn_raw_buffer_load_b128(rws, off, 0, 16);
      }
#pragma unroll
      for (int q = 0; q < 16; q++) ok = ok & (j0 - 2 * q < 0 || recok(g[q], c.tlo, c.thi));
      if (__all(ok) || spins > SPIN_CAP) break;
      __builtin_amdgcn_s_sleep(1);
    }
#pragma unroll
    for (int q = 15; q >= 0; q--) S = cfma(l128, S, cf{__uint_as_float(g[q].x), __uint_as_float(g[q].z)});
  }
  cf S0, S1;
  half_both(S, S0, S1);
  const cf C = cfma(l64, S1, S0);
  lds_w<f32x2>(scan_scr(mh) + l31 * 8, f32x2{C.r, C.i});
  wait_lgkm();
  lds_w<u32>(LFLAG + mh * 4, 1u);
}

struct Frag { bf16x8 x[2]; bf16x8 wv[5]; };
struct Scan { f32x2 ur[16], ui[16], ar, ai, an; cf pa, pb, za, zb; };
template <int NH>
DEVI void compute(const Params& p, Ctx& c, Scan& sc STAMP_ARG) {
  const int w = c.w, lane = c.lane, rp = c.rp, cg = c.cg;
  const int mh = w >> 1;
  const int l31 = lane & 31, h = lane >> 5;
  const __amdgpu_buffer_rsrc_t rws = __builtin_amdgcn_make_buffer_rsrc(p.ws, 0, WS_BYTES, 0x00020000);
  const v2u pe = __builtin_amdgcn_raw_buffer_load_b64(rws, WS_EPOCH, 0, 16);
  cf lam, s2, l16;
  if constexpr (NH == 0) {
    const int ch = cg * 32 + l31;
    const float lr = bf2f(p.lre[ch]), li = bf2f(p.lim[ch]), ld = bf2f(p.ldt[ch]);
    const u32 tch = __builtin_amdgcn_raw_buffer_load_b32(
        mh ? rws : __builtin_amdgcn_make_buffer_rsrc(p.out, 0, L * D * 4, 0x00020000), lane < 8 ? (u32)lane << 21 : OOB, 0, 0);
    const float dt = (float)(__bf16)expf(ld);
    const float er = expf(dt * lr);
    float sn, cn;
    sincosf(dt * li, &sn, &cn);
    lam = cf{smul(er, cn), smul(er, sn)};
    const float inv = 2.f / sfma(lr, lr, smul(li, li));
    const cf nq = cmul(cf{lam.r - 1.f, lam.i}, cf{lr, -li});
    s2 = cf{smul(nq.r, inv), smul(nq.i, inv)};
    const cf l2 = cmul(lam, lam), l4 = cmul(l2, l2), l8 = cmul(l4, l4);
    l16 = cmul(l8, l8);
    const cf l32 = cmul(l16, l16), l64 = cmul(l32, l32), l128 = cmul(l64, l64);
    lds_w<f32x4>(POW + l31 * 16, f32x4{l64.r, l64.i, l128.r, l128.i});
    make_tag(c, pe);
    asm volatile("" ::"v"(tch));
  } else {
    const int row0 = rp * 128 + cg * 8 + mh * 4;
    v4u xr[4];
#pragma unroll
    for (int k = 0; k < 4; k++) xr[k] = *(const v4u*)(p.x + (row0 + k) * D + lane * 8);
    make_tag(c, pe);
    float mo = 0.f, ro = 0.f;
#pragma unroll
    for (int k = 0; k < 4; k++) {
      float v[8];
#pragma unroll
      for (int i = 0; i < 4; i++) { v[2 * i] = bf2f(xr[k][i]); v[2 * i + 1] = bfhi(xr[k][i]); }
      const float m = wave_sum(((v[0] + v[1]) + (v[2] + v[3])) + ((v[4] + v[5]) + (v[6] + v[7]))) * (1.f / D);
      float q = 0.f;
#pragma unroll
      for (int i = 0; i < 8; i++) { const float d = v[i] - m; q = sfma(d, d, q); }
      q = wave_sum(q);
      if (lane == k) { mo = m; ro = q; }
    }
    ro = __builtin_amdgcn_rsqf(ro * (1.f / D) + 1e-5f);
    if (lane < 4) __builtin_amdgcn_raw_buffer_store_b128(mkrec(mo, ro, c.tlo, c.thi), rws, WS_RS + (row0 + lane) * 16, 0, 16);
  }
  const u32 tlo = c.tlo, thi = c.thi;
  u32 xa[4];
#pragma unroll
  for (int ks = 0; ks < 4; ks++) xa[ks] = frag_xa(lane, ks);
  const u32 wa = frag_wa(lane);

  f32x16 acc[9];
#pragma unroll
  for (int k = 0; k < 9; k++)
#pragma unroll
    for (int i = 0; i < 16; i++) acc[k][i] = 0.f;

  auto mm = [&](const Frag& f, int i) {
    if constexpr (NH == 0) {
      switch (i) {
        case 0: acc[0] = mfma(f.x[0], f.wv[0], acc[0]); break;
        case 1: acc[1] = mfma(f.x[0], f.wv[1], acc[1]); break;
        case 2: acc[3] = mfma(f.x[1], f.wv[1], acc[3]); break;
        case 3: acc[2] = mfma(f.x[1], f.wv[0], acc[2]); break;
        case 4: acc[5] = mfma(f.wv[2], f.x[1], acc[5]); break;
        case 5: acc[4] = mfma(f.wv[2], f.x[0], acc[4]); break;
        case 6: acc[6] = mfma(f.wv[3], f.x[0], acc[6]); break;
        case 7: acc[7] = mfma(f.wv[3], f.x[1], acc[7]); break;
        default: acc[8] = mfma(f.wv[4], f.x[0], acc[8]); break;
      }
    } else {
      if (i < 8) {
        const int t = i >> 1, mi = (i ^ t) & 1;
        acc[2 * t + mi] = mfma(f.wv[t], f.x[mi], acc[2 * t + mi]);
      } else {
        acc[8] = mfma(f.wv[4], f.x[1], acc[8]);
      }
    }
  };
  auto rd = [&](Frag& f, int q, u32 sb_, int ks) {
    const u32 ax = sb_ + xa[ks] + mh * 8192, aw = sb_ + wa + ks * 3584;
    if constexpr (NH == 0) {
      const u32 ab = sb_ + xa[ks] + BSO;
      if (q == 0) { f.x[0] = lds_r<bf16x8>(ax); f.wv[0] = lds_r<bf16x8>(ab); }
      if (q == 1) { f.wv[1] = lds_r<bf16x8>(ab + 4096); f.x[1] = lds_r<bf16x8>(ax + 4096); }
      if (q == 2) f.wv[2] = lds_tr2(aw, aw + 896);
      if (q == 3) f.wv[3] = lds_tr2(aw + 32, aw + 32 + 896);
      if (q == 4) f.wv[4] = lds_tr2(aw + 192, aw + 192 + 896);
    } else {
      if (q == 0) { f.x[0] = lds_r<bf16x8>(ax); f.x[1] = lds_r<bf16x8>(ax + 4096); }
      if (q >= 1 && q <= 5) f.wv[q - 1] = lds_tr2(aw + 32 + q * 32, aw + 32 + q * 32 + 896);
    }
  };
  auto step = [&](const Frag& cur, Frag& nxt, u32 sb_, int ks) {
#pragma unroll
    for (int i = 0; i < 9; i++) {
      mm(cur, i);
      SBAR();
      if (i < 6) { rd(nxt, i, sb_, ks); SBAR(); }
    }
  };

  Frag fa, fb;
#pragma unroll
  for (int k = 0; k < 9; k++) asm volatile("" : "+v"(acc[k]));
  STAMP(1);
  lds_bar();
  STAMP(2);
#pragma unroll
  for (int q = 0; q < 6; q++) rd(fa, q, 0, 0);
  u32 sbase = 0;
  STAMP(12);
#pragma unroll 1
  for (int s = 0; s < 8; s++) {
    SBAR();
    step(fa, fb, sbase, 1);
    step(fb, fa, sbase, 2);
    step(fa, fb, sbase, 3);
    sbase = sbase == 2 * SLOT ? 0 : sbase + SLOT;
    mm(fb, 0);
    mm(fb, 1);
    SBAR();
    lds_bar();
#pragma unroll
    for (int i = 2; i < 9; i++) {
      if (i < 8) { rd(fa, i - 2, sbase, 0); SBAR(); }
      mm(fb, i);
      SBAR();
    }
  }
  STAMP(13);
  float rr[2];
  f32x4 ra[4], rb[4];
  {
    const u32 ax = h ? ZERO : XC + (mh * 64 + l31) * 16;
    fa.x[0] = lds_r<bf16x8>(ax);
    fa.x[1] = lds_r<bf16x8>(h ? ZERO : ax + 512);
    if constexpr (NH == 0) {
      fa.wv[0] = lds_r<bf16x8>(WC + 7 * 512 + l31 * 16);
      fa.wv[1] = lds_r<bf16x8>(WC + 8 * 512 + l31 * 16);
      fa.wv[2] = lds_r<bf16x8>(WC + l31 * 16);
      fa.wv[3] = lds_r<bf16x8>(WC + 512 + l31 * 16);
    } else {
#pragma unroll
      for (int t = 0; t < 4; t++) fa.wv[t] = lds_r<bf16x8>(WC + (2 + t) * 512 + l31 * 16);
    }
    fa.wv[4] = lds_r<bf16x8>(WC + 6 * 512 + l31 * 16);
    SBAR();
#pragma unroll
    for (int mi = 0; mi < 2; mi++) rr[mi] = lds_r<float>(RT_R + (u32)(mh * 64 + mi * 32 + tau(l31)) * 4);
    if constexpr (NH == 0) {
#pragma unroll
      for (int q = 0; q < 4; q++) {
        ra[q] = lds_r<f32x4>(RT_R + (u32)(mh * 64 + h * 16 + q * 4) * 4);
        rb[q] = lds_r<f32x4>(RT_R + (u32)(mh * 64 + 32 + h * 16 + q * 4) * 4);
      }
    }
    SBAR();
#pragma unroll
    for (int i = 0; i < 9; i++) { mm(fa, i); SBAR(); }
  }
  STAMP(3);

  const int r8 = lane >> 3, c8 = lane & 7;
  const u32 vf = (u32)((224 * w + r8) * 1024 + c.ct * 128 + ((c8 ^ (((r8 >> 1) & 1) << 2)) << 4));
  auto issue_v10 = [&]() {
    const v4u s2w = make_srd(p.w2, F * D * 2);
    glds16x4(upos(6, w), vf, vf + 7168, vf + 14336, vf + 21504, s2w, 2 * 49152);
    glds16x2(upos(8, w), vf, vf + 7168, s2w, 2 * 49152 + 32768);
  };
  const int mt0 = rp * 4 + mh * 2;
  const u32 fo = WS_FLAG + ((rp * 2 + mh) * 16 + cg) * 32;
  f32x2 ke[2], iv[2];
#pragma unroll
  for (int mi = 0; mi < 2; mi++) {
    ke[mi] = dup(rr[mi] * -1.4426950408889634f);
    iv[mi] = dup(__builtin_amdgcn_rcpf(rr[mi] * rr[mi]));
  }
  auto ffn_tile = [&](const f32x16& a, int mi, int j) {
    const f32x2 kk = ke[mi], ii = iv[mi];
    u32 o[4];
#pragma unroll
    for (int q = 0; q < 2; q++)
#pragma unroll
      for (int e = 0; e < 4; e += 2) {
        const f32x2 g = {a[8 * q + e], a[8 * q + e + 1]}, u = {a[8 * q + 4 + e], a[8 * q + 5 + e]};
        const f32x2 ta = g * kk;
        const f32x2 ex = {__builtin_amdgcn_exp2f(ta.x), __builtin_amdgcn_exp2f(ta.y)};
        const f32x2 dn = fma2(ex, ii, ii);
        const f32x2 rc = {__builtin_amdgcn_rcpf(dn.x), __builtin_amdgcn_rcpf(dn.y)};
        const f32x2 hv = (g * u) * rc;
        o[2 * q + (e >> 1)] = pk2(hv.x, hv.y);
      }
    __builtin_amdgcn_raw_buffer_store_b128(v4u{o[0], o[1], o[2], o[3]}, rws, WS_H + lane * 16 + ((mt0 + mi) * 176 + cg * 11 + j) * 1024, 0, 16);
  };
  if constexpr (NH == 1) {
    issue_v10();
#pragma unroll
    for (int t = 0; t < 4; t++) { ffn_tile(acc[2 * t], 0, 2 + t); ffn_tile(acc[2 * t + 1], 1, 2 + t); }
    ffn_tile(acc[8], 1, 6);
    wait_vm<0>();
    if (lane == 0) __builtin_amdgcn_raw_buffer_store_b64(v2u{tlo, thi}, rws, fo + 8, 0, 16);
  } else {
    const f32x2 ar = dup(lam.r), ai = dup(lam.i), an = dup(-lam.i);
    f32x2 er = {0.f, 0.f}, ei = {0.f, 0.f};
#pragma unroll
    for (int i = 0; i < 16; i++) {
      sc.ur[i] = f32x2{smul(acc[0][i], ra[i >> 2][i & 3]), smul(acc[2][i], rb[i >> 2][i & 3])};
      sc.ui[i] = f32x2{smul(acc[1][i], ra[i >> 2][i & 3]), smul(acc[3][i], rb[i >> 2][i & 3])};
      const f32x2 nr = fma2(ar, er, fma2(an, ei, sc.ur[i]));
      ei = fma2(ar, ei, fma2(ai, er, sc.ui[i]));
      er = nr;
    }
    cf e0, e1, e2, e3;
    half_both(cf{er.x, ei.x}, e0, e1);
    half_both(cf{er.y, ei.y}, e2, e3);
    const cf z2 = cfma(l16, e0, e1), z3 = cfma(l16, z2, e2), G = cfma(l16, z3, e3);
    if (h == 0)
      __builtin_amdgcn_raw_buffer_store_b128(mkrec(G.r, G.i, tlo, thi), rws, WS_AGG + (cg * 32 + l31) * 16 + (rp * 2 + mh) * 8192, 0, 16);
    SBAR();
    STAMP(4);
    issue_v10();
    ffn_tile(acc[4], 0, 0); ffn_tile(acc[5], 1, 0);
    ffn_tile(acc[6], 0, 1); ffn_tile(acc[7], 1, 1);
    ffn_tile(acc[8], 0, 6);
    SBAR();
    {
      const f32x2 sr = dup(s2.r), si = dup(s2.i), sn = dup(-s2.i);
#pragma unroll
      for (int i = 0; i < 16; i++) {
        const f32x2 nr = fma2(sr, sc.ur[i], sn * sc.ui[i]);
        sc.ui[i] = fma2(sr, sc.ui[i], si * sc.ur[i]);
        sc.ur[i] = nr;
      }
    }
    const cf l32 = cmul(l16, l16), l48 = cmul(l32, l16);
    sc.pa = cmul(s2, h ? l16 : cf{1.f, 0.f}); sc.pb = cmul(s2, h ? l48 : l32);
    sc.za = cmul(s2, h ? e0 : cf{0.f, 0.f}); sc.zb = cmul(s2, h ? z3 : z2);
    sc.ar = ar; sc.ai = ai; sc.an = an;
    SBAR();
    wait_vm<0>();
    if (lane == 0) __builtin_amdgcn_raw_buffer_store_b64(v2u{tlo, thi}, rws, fo, 0, 16);
  }
  STAMP(6);
}

DEVI void scan_finish(const Params& p, const Ctx& c, const Scan& sc STAMP_ARG) {
  const int lane = c.lane, mh = c.w >> 1, l31 = lane & 31, h = lane >> 5;
  const __amdgpu_buffer_rsrc_t rws = __builtin_amdgcn_make_buffer_rsrc(p.ws, 0, WS_BYTES, 0x00020000);
  const u32 scr = scan_scr(mh);
  lds_spin(LFLAG + mh * 4);
  STAMP(5);
  const f32x2 Cv = lds_r<f32x2>(scr + l31 * 8);
  const cf C = {Cv.x, Cv.y};
  const cf ha = cfma(sc.pa, C, sc.za), hb = cfma(sc.pb, C, sc.zb);
  f32x2 hr = {ha.r, hb.r}, hi = {ha.i, hb.i};
  const f32x2 ar = sc.ar, ai = sc.ai, an = sc.an;
  const u32 wb = scr + l31 * 64;
  const u32 fsw = (l31 >> 1) & 3;
  const int g1 = (lane >> 4) & 1, a = (lane & 15) >> 2, pp = lane & 3;
  const int cki = 2 * (pp & 1) + g1;
  const u32 r0 = scr + (8 * h + a) * 64 + ((cki ^ (a >> 1)) << 4) + 8 * (pp >> 1);
  const u32 r1 = scr + (8 * h + 4 + a) * 64 + ((cki ^ (2 + (a >> 1))) << 4) + 8 * (pp >> 1);
  const int mt0 = c.rp * 4 + mh * 2;
  u32 pr[2][8], pi[2][8];
#pragma unroll
  for (int i = 0; i < 16; i += 2) {
    const f32x2 tr = fma2(ar, hr, fma2(an, hi, sc.ur[i])), ti = fma2(ar, hi, fma2(ai, hr, sc.ui[i]));
    hr = fma2(ar, tr, fma2(an, ti, sc.ur[i + 1]));
    hi = fma2(ar, ti, fma2(ai, tr, sc.ui[i + 1]));
    pr[0][i >> 1] = pk2(tr.x, hr.x); pr[1][i >> 1] = pk2(tr.y, hr.y);
    pi[0][i >> 1] = pk2(-ti.x, -hi.x); pi[1][i >> 1] = pk2(-ti.y, -hi.y);
  }
#pragma unroll
  for (int mi = 0; mi < 2; mi++) {
#pragma unroll
    for (int q = 0; q < 2; q++) {
      const u32 co = ((2 * h + q) ^ fsw) << 4;
      lds_w<v4u>(wb + co, v4u{pr[mi][4 * q], pr[mi][4 * q + 1], pr[mi][4 * q + 2], pr[mi][4 * q + 3]});
      lds_w<v4u>(wb + 2048 + co, v4u{pi[mi][4 * q], pi[mi][4 * q + 1], pi[mi][4 * q + 2], pi[mi][4 * q + 3]});
    }
#pragma unroll
    for (int q = 0; q < 4; q++) {
      const bf16x8 v = lds_tr2(r0 + q * 1024, r1 + q * 1024);
      __builtin_amdgcn_raw_buffer_store_b128(__builtin_bit_cast(v4u, v), rws,
                                             WS_H + lane * 16 + ((mt0 + mi) * 176 + c.cg * 11 + 7 + q) * 1024, 0, 16);
    }
  }
  wait_vm<0>();
  if (lane == 0)
    __builtin_amdgcn_raw_buffer_store_b64(v2u{c.tlo, c.thi}, rws, WS_FLAG + ((c.rp * 2 + mh) * 16 + c.cg) * 32 + 16, 0, 16);
  STAMP(14);
}

__global__ __launch_bounds__(512, 2) void fused(Params p) {
  __shared__ __attribute__((aligned(16))) char smem[LDS_BYTES];
  asm volatile("" ::"s"((u32)(uintptr_t)(LAS char*)smem));
  const int b = blockIdx.x, xcd = b & 7, bi = b >> 3;
  const int rp = (xcd & 1) * 8 + (bi & 7), cg = (xcd >> 1) * 4 + (bi >> 3);
  const int rt = xcd * 4 + (bi >> 3), ct = bi & 7;
  const int w = __builtin_amdgcn_readfirstlane(threadIdx.x >> 6), lane = threadIdx.x & 63;
  const int l31 = lane & 31, h = lane >> 5;
  STAMP_DECL;
  STAMP(0);
  const int r8 = lane >> 3, c8 = lane & 7;
  const u32 vf = (u32)((224 * w + r8) * 1024 + ct * 128 + ((c8 ^ (((r8 >> 1) & 1) << 2)) << 4));
  Ctx c;
  c.w = w; c.lane = lane; c.rp = rp; c.cg = cg; c.ct = ct;
  Scan sc;
  if (w >= 4) {
    loader(p, c STAMP_PASS);
    const v4u s2w = make_srd(p.w2, F * D * 2);
    glds16x4(upos(6, w), vf, vf + 7168, vf + 14336, vf + 21504, s2w, 2 * 49152);
    glds16x2(upos(8, w), vf, vf + 7168, s2w, 2 * 49152 + 32768);
    wait_vm<6>();
    lds_w<u32>(LFLAG + 8 + (w - 4) * 4, 1u);
    wait_vm<0>();
  } else {
    if (w & 1) compute<1>(p, c, sc STAMP_PASS);
    else compute<0>(p, c, sc STAMP_PASS);
    lds_spin(LFLAG + 8 + w * 4);
  }
  const u32 tlo = c.tlo, thi = c.thi;

  const v4u s2w = make_srd(p.w2, F * D * 2), scr = make_srd(p.cre, D * P * 2), sci = make_srd(p.cim, D * P * 2);
  const __amdgpu_buffer_rsrc_t rws = __builtin_amdgcn_make_buffer_rsrc(p.ws, 0, WS_BYTES, 0x00020000);
  const u32 vc0 = (u32)((64 * ct + r8) * 1024 + w * 128 + ((c8 ^ (r8 >> 1)) << 4)), vc1 = vc0 ^ 64u;
  const int g1 = (lane >> 4) & 1, a = (lane & 15) >> 2, pp = lane & 3;
  const u32 fa0 = (u32)((4 * h + a) * 128 + ((4 * (a >> 1) + 2 * g1 + (pp >> 1)) << 4) + 8 * (pp & 1)), fa1 = fa0 ^ 64u;
  auto issue_unit = [&](int u, int slot) {
    const u32 dst = upos(slot, w);
    if (u < 14) {
      glds16(dst, vf, s2w, u * 16384);
      glds16(dst + 1024, vf, s2w, u * 16384 + 8192);
    } else {
      const int v = u - 14;
      glds16(dst, vc0, (v >> 2) ? sci : scr, (v & 3) * 16384);
      glds16(dst + 1024, vc1, (v >> 2) ? sci : scr, (v & 3) * 16384 + 8192);
    }
  };
  auto read_unit = [&](bf16x8 (&d)[2], int slot) {
    const u32 base = upos(slot, w);
    d[0] = lds_tr2(base + fa0, base + fa0 + 1024);
    d[1] = lds_tr2(base + fa1, base + fa1 + 1024);
  };
  bf16x8 Bf[13][2];
  wait_vm<1>();
#pragma unroll
  for (int q = 0; q < 9; q++) read_unit(Bf[q], q);
  wait_lgkm();
#pragma unroll
  for (int q = 0; q < 9; q++) issue_unit(9 + q, q);
  if (w == 4 || w == 5) carry_job(p, c, w - 4);
  wait_vm<10>();
#pragma unroll
  for (int q = 0; q < 4; q++) read_unit(Bf[9 + q], q);
  wait_lgkm();
#pragma unroll
  for (int q = 0; q < 4; q++) issue_unit(18 + q, q);
  STAMP(7);
  if (w < 4 && !(w & 1)) scan_finish(p, c, sc STAMP_PASS);

  const u32 fo = lane < 8 ? WS_FLAG + (u32)((rt * 16 + 2 * w) * 4 + lane) * 8 : OOB;
  auto poll = [&](bool need) {
    for (u32 spins = 0;; spins++) {
      const v2u f = __builtin_amdgcn_raw_buffer_load_b64(rws, fo, 0, 16);
      if (__all(!need || (f.x == tlo && f.y == thi)) || spins > SPIN_CAP) break;
      __builtin_amdgcn_s_sleep(1);
    }
    asm volatile("" ::: "memory");
  };
  poll(lane < 8 && (lane & 3) < 2);
  STAMP(8);

  f32x16 acc[2][2];
#pragma unroll
  for (int k = 0; k < 16; k++) { acc[0][0][k] = 0.f; acc[0][1][k] = 0.f; acc[1][0][k] = 0.f; acc[1][1][k] = 0.f; }
  const u32 hb = WS_H + lane * 16;
  const u32 hs0 = (u32)((2 * rt * 176 + 22 * w) * 1024), hs1 = hs0 + 176 * 1024;
  auto lda = [&](int kb, int mi) {
    return __builtin_bit_cast(bf16x8, __builtin_amdgcn_raw_buffer_load_b128(rws, hb + kb * 1024, mi ? hs1 : hs0, 16));
  };
  constexpr int NST = 22, WIN = 8;
  auto kb_of = [](int st) {
    if (st < 14) return (st / 7) * 11 + (st % 7);
    const int u = st - 14, mat = u >> 2, kb4 = u & 3;
    return (kb4 >> 1) * 11 + 7 + 2 * mat + (kb4 & 1);
  };
  bf16x8 Af[NST][2];
#pragma unroll
  for (int st = 0; st < WIN; st++) { Af[st][0] = lda(kb_of(st), 0); Af[st][1] = lda(kb_of(st), 1); }
  SBAR();
  const u32 csw = (u32)((l31 & 15) * 128 + ((h ^ ((l31 >> 1) & 7)) << 4));
  const bool lo16 = l31 < 16;
  v2u f2;
#pragma unroll
  for (int st = 0; st < NST; st++) {
    bf16x8 b0, b1;
    if (st < 13) { b0 = Bf[st][0]; b1 = Bf[st][1]; }
    else if (st == 13) {
      bf16x8 tb[2];
      read_unit(tb, 4);
      b0 = tb[0]; b1 = tb[1];
    } else {
      const int u = st - 14, mat = u >> 2, kb4 = u & 3;
      const int s0 = (5 + 4 * mat) % 9, s1 = (6 + 4 * mat) % 9, s2 = (7 + 4 * mat) % 9, s3 = (8 + 4 * mat) % 9;
      const u32 o = csw ^ (u32)(kb4 << 5);
      b0 = lds_r<bf16x8>((lo16 ? upos(s0, w) : upos(s1, w)) + o);
      b1 = lds_r<bf16x8>((lo16 ? upos(s2, w) : upos(s3, w)) + o);
    }
    acc[0][0] = mfma(b0, Af[st][0], acc[0][0]);
    acc[0][1] = mfma(b1, Af[st][0], acc[0][1]);
    acc[1][1] = mfma(b1, Af[st][1], acc[1][1]);
    acc[1][0] = mfma(b0, Af[st][1], acc[1][0]);
    if (st == 3) f2 = __builtin_amdgcn_raw_buffer_load_b64(rws, fo, 0, 16);
    if (st == 6) {
      const bool need = lane < 8 && (lane & 3) == 2;
      if (!__all(!need || (f2.x == tlo && f2.y == thi))) poll(need);
      asm volatile("" ::: "memory");
    }
    if (st + WIN < NST) { Af[st + WIN][0] = lda(kb_of(st + WIN), 0); Af[st + WIN][1] = lda(kb_of(st + WIN), 1); }
    SBAR();
  }
  STAMP(9);
  const int tid = threadIdx.x;
  v2u xv[2]; v4u st2[2];
  const int dq = tid & 15;
  const v2u dk = *(const v2u*)(p.dsk + ct * 64 + dq * 4);
  int grow[2];
#pragma unroll
  for (int e = 0; e < 2; e++) {
    const int rr = (tid >> 4) + 32 * e;
    grow[e] = rt * 64 + (rr & 32) + tau(rr & 31);
    xv[e] = *(const v2u*)(p.x + grow[e] * D + ct * 64 + dq * 4);
    st2[e] = __builtin_amdgcn_raw_buffer_load_b128(rws, WS_RS + grow[e] * 16, 0, 16);
  }
  wait_lgkm();
#pragma unroll
  for (int mi = 0; mi < 2; mi++)
#pragma unroll
    for (int cc = 0; cc < 2; cc++)
#pragma unroll
      for (int j = 0; j < 4; j++) {
        const int q = (mi * 2 + cc) * 4 + j;
        const u32 pos = (u32)(lane ^ (cc * 8 + j * 2 + h));
        lds_w<f32x4>((q / 6) * SLOT + (q % 6) * 1024 + w * 6144 + pos * 16,
                     f32x4{acc[mi][cc][4 * j], acc[mi][cc][4 * j + 1], acc[mi][cc][4 * j + 2], acc[mi][cc][4 * j + 3]});
      }
  lds_bar();
  STAMP(10);
  if (b == 255 - 56 && tid == 0)
    __builtin_amdgcn_raw_buffer_store_b64(v2u{tlo, thi}, rws, WS_EPOCH, 0, 16);
#pragma unroll
  for (int e = 0; e < 2; e++) {
    const int rho = (tid >> 4) & 31;
    const int q = e * 8 + (dq >> 1), q6 = (q * 11) >> 6;
    const u32 ad = (u32)(q6 * SLOT + (q - 6 * q6) * 1024 + ((rho + 32 * (dq & 1)) ^ dq) * 16);
    f32x4 s = lds_r<f32x4>(ad);
#pragma unroll
    for (int k = 1; k < 8; k++) s += lds_r<f32x4>(ad + k * 6144);
    const float m = __uint_as_float(st2[e].x), r = __uint_as_float(st2[e].z);
    const float x0 = bf2f(xv[e].x), x1 = bfhi(xv[e].x), x2 = bf2f(xv[e].y), x3 = bfhi(xv[e].y);
    const float d0 = bf2f(dk.x) * r, d1 = bfhi(dk.x) * r, d2 = bf2f(dk.y) * r, d3 = bfhi(dk.y) * r;
    f32x4 o;
    o[0] = s[0] + x0 + (x0 - m) * d0;
    o[1] = s[1] + x1 + (x1 - m) * d1;
    o[2] = s[2] + x2 + (x2 - m) * d2;
    o[3] = s[3] + x3 + (x3 - m) * d3;
    *(f32x4*)(p.out + grow[e] * D + ct * 64 + dq * 4) = o;
  }
  STAMP(11);
  STAMP_FLUSH(0, 16);
}

extern "C" void kernel_launch(void* const* d_in, const int* in_sizes, int n_in, void* d_out, int out_size, void* d_ws,
                              size_t ws_size, hipStream_t stream) {
  Params p;
  p.x = (const u16*)d_in[0];
  p.lre = (const u16*)d_in[1];
  p.lim = (const u16*)d_in[2];
  p.bre = (const u16*)d_in[3];
  p.bim = (const u16*)d_in[4];
  p.cre = (const u16*)d_in[5];
  p.cim = (const u16*)d_in[6];
  p.ldt = (const u16*)d_in[7];
  p.dsk = (const u16*)d_in[8];
  p.w1 = (const u16*)d_in[10];
  p.w3 = (const u16*)d_in[11];
  p.w2 = (const u16*)d_in[12];
  p.out = (float*)d_out;
  p.ws = (char*)d_ws;
  fused<<<256, 512, 0, stream>>>(p);
}
```

```cpp
#include <hip/hip_runtime.h>
#include <stdint.h>

typedef unsigned short u16;
typedef unsigned int u32;
typedef unsigned long long u64;
typedef __attribute__((ext_vector_type(8))) __bf16 bf16x8;
typedef __attribute__((ext_vector_type(4))) __bf16 bf16x4;
typedef __attribute__((ext_vector_type(2))) __bf16 bf16x2;
typedef __attribute__((ext_vector_type(16))) float f32x16;
typedef __attribute__((ext_vector_type(4))) float f32x4;
typedef __attribute__((ext_vector_type(2))) float f32x2;
typedef __attribute__((ext_vector_type(4))) u32 v4u;
typedef __attribute__((ext_vector_type(2))) u32 v2u;
#define LAS __attribute__((address_space(3)))
#define DEVI __device__ __forceinline__
#define SBAR() __builtin_amdgcn_sched_barrier(0)

constexpr int L = 2048, D = 512, P = 512, F = 1792;

constexpr u32 WS_EPOCH = 0;
constexpr u32 WS_RS = 4096;
constexpr u32 WS_CS = WS_RS + 2048 * 16;
constexpr u32 WS_AGG = WS_CS + 18 * 8 * 128 * 16;
constexpr u32 WS_FLF = WS_AGG + 16 * 512 * 16;
constexpr u32 WS_FLS = WS_FLF + 13 * 56 * 128;
constexpr u32 WS_H = 2u << 20;
constexpr u32 H_BYTES = 64u * 176u * 1024u;
constexpr u32 WS_BYTES = WS_H + H_BYTES;
constexpr u32 OOB = 0x10000000u;
constexpr u32 CODE_BYTES = 52224;

constexpr u32 XO = 0, W1O = 20480, W3O = W1O + 16384 + 128, SLOT = W3O + 16384;
constexpr u32 TAIL = 49152;
constexpr u32 WC = 2 * SLOT + TAIL, XC = WC + 4096, ZERO = WC + 7680, LFLAG = ZERO + 16, TAGW = LFLAG + 112;
constexpr u32 F_FILL = LFLAG, F_TAB = LFLAG + 16, F_OUT = LFLAG + 32, F_CONS = LFLAG + 48, F_DONE = LFLAG + 64, F_RDY = LFLAG + 80;
static_assert(ZERO >= 3 * SLOT && TAGW + 8 <= 163840, "lds map");
constexpr u32 LDS_BYTES = 163840;
constexpr u32 SPIN_CAP = 1u << 16;
constexpr int PFA = 6, PFB = 10, NPRE = 52;
constexpr int AINIT = 4, AWIN = 7;
constexpr int ODDAT = 7, SSMAT = 28 - AWIN, SFAT = SSMAT - AWIN + 3, HALFAT = 2, LASTAT = 4;
constexpr int POLLSLEEP = 3, WDELAY = 24;

struct Params {
  const u16 *x, *lre, *lim, *bre, *bim, *cre, *cim, *ldt, *dsk, *w1, *w3, *w2;
  float* out;
  char* ws;
};

DEVI v4u make_srd(const void* p, u32 bytes) {
  u64 a = (u64)p;
  return v4u{(u32)a, (u32)(a >> 32) & 0xffffu, bytes, 0x00020000u};
}
DEVI void glds16(u32 lds, u32 voff, v4u srd, u32 soff) {
  lds = __builtin_amdgcn_readfirstlane(lds);
  asm volatile("s_mov_b32 m0, %0\n\ts_nop 0\n\tbuffer_load_dwordx4 %1, %2, %3 offen lds" ::"s"(lds), "v"(voff), "s"(srd), "s"(soff)
               : "memory");
}
template <int N> DEVI void wait_vm_bar() {
  asm volatile("s_waitcnt vmcnt(%0) lgkmcnt(0)\n\ts_barrier" ::"n"(N) : "memory");
}
DEVI void lds_bar() { asm volatile("s_waitcnt lgkmcnt(0)\n\ts_barrier" ::: "memory"); }
template <int N> DEVI void wait_vm() { asm volatile("s_waitcnt vmcnt(%0)" ::"n"(N) : "memory"); }
DEVI void wait_lgkm() { asm volatile("s_waitcnt lgkmcnt(0)" ::: "memory"); }
DEVI float bf2f(u32 v) { return __uint_as_float(v << 16); }
DEVI float bfhi(u32 v) { return __uint_as_float(v & 0xffff0000u); }
template <class T> DEVI T lds_r(u32 a) { return *(LAS T*)(uintptr_t)a; }
template <class T> DEVI void lds_w(u32 a, T v) { *(LAS T*)(uintptr_t)a = v; }
DEVI bf16x8 lds_tr2(u32 a0, u32 a1) {
  bf16x4 lo = __builtin_amdgcn_ds_read_tr16_b64_v4bf16((LAS bf16x4*)(uintptr_t)a0);
  bf16x4 hi = __builtin_amdgcn_ds_read_tr16_b64_v4bf16((LAS bf16x4*)(uintptr_t)a1);
  return __builtin_shufflevector(lo, hi, 0, 1, 2, 3, 4, 5, 6, 7);
}
DEVI f32x16 mfma(bf16x8 a, bf16x8 b, f32x16 c) { return __builtin_amdgcn_mfma_f32_32x32x16_bf16(a, b, c, 0, 0, 0); }
typedef __attribute__((ext_vector_type(4))) short s16x4;
DEVI f32x4 sum4(bf16x4 v, f32x4 c) {
  return __builtin_amdgcn_mfma_f32_4x4x4bf16_1k(s16x4{0x3f80, 0x3f80, 0x3f80, 0x3f80}, __builtin_bit_cast(s16x4, v), c, 0, 0, 0);
}
template <int CTRL> DEVI float dppx(float v) {
  return __int_as_float(__builtin_amdgcn_update_dpp(0, __float_as_int(v), CTRL, 0xf, 0xf, true));
}
DEVI float half_sum(float v) {
  auto r = __builtin_amdgcn_permlane32_swap(__float_as_uint(v), __float_as_uint(v), false, false);
  return __uint_as_float(r[0]) + __uint_as_float(r[1]);
}
DEVI float wave_sum(float v) {
  v += dppx<0xB1>(v);
  v += dppx<0x4E>(v);
  v += dppx<0x141>(v);
  v += dppx<0x140>(v);
  auto r = __builtin_amdgcn_permlane16_swap(__float_as_uint(v), __float_as_uint(v), false, false);
  return half_sum(__uint_as_float(r[0]) + __uint_as_float(r[1]));
}
DEVI u32 pk2(float a, float b) {
  bf16x2 v = {(__bf16)a, (__bf16)b};
  return __builtin_bit_cast(u32, v);
}
DEVI int tau(int r) { return 16 * ((r >> 2) & 1) + 4 * (r >> 3) + (r & 3); }
DEVI f32x2 fma2(f32x2 a, f32x2 b, f32x2 c) { return __builtin_elementwise_fma(a, b, c); }
DEVI f32x2 dup(float v) { f32x2 r = {v, v}; asm("" : "+v"(r)); return r; }
DEVI float smul(float a, float b) { float r; asm("v_mul_f32 %0, %1, %2" : "=v"(r) : "v"(a), "v"(b)); return r; }
DEVI float sfma(float a, float b, float c) { float r; asm("v_fma_f32 %0, %1, %2, %3" : "=v"(r) : "v"(a), "v"(b), "v"(c)); return r; }
DEVI float sfms(float a, float b, float c) { float r; asm("v_fma_f32 %0, -%1, %2, %3" : "=v"(r) : "v"(a), "v"(b), "v"(c)); return r; }
struct cf { float r, i; };
DEVI cf cfma(cf a, cf b, cf c) { return cf{sfma(a.r, b.r, sfms(a.i, b.i, c.r)), sfma(a.r, b.i, sfma(a.i, b.r, c.i))}; }
DEVI cf cmul(cf a, cf b) { return cf{sfms(a.i, b.i, smul(a.r, b.r)), sfma(a.r, b.i, smul(a.i, b.r))}; }
DEVI void half_both(cf v, cf& lo, cf& hi) {
  asm volatile("s_nop 1" : "+v"(v.r), "+v"(v.i));
  auto r = __builtin_amdgcn_permlane32_swap(__float_as_uint(v.r), __float_as_uint(v.r), false, false);
  auto q = __builtin_amdgcn_permlane32_swap(__float_as_uint(v.i), __float_as_uint(v.i), false, false);
  lo = cf{__uint_as_float(r[0]), __uint_as_float(q[0])};
  hi = cf{__uint_as_float(r[1]), __uint_as_float(q[1])};
}
DEVI v4u mkrec(float a, float b, u32 tlo, u32 thi) {
  const u32 x = __float_as_uint(a), y = __float_as_uint(b);
  return v4u{x, tlo ^ y, y, thi ^ x};
}
DEVI bool recok(v4u g, u32 tlo, u32 thi) { return (g.y ^ g.z) == tlo && (g.w ^ g.x) == thi; }


DEVI constexpr u32 cell0(int i) { return (u32)(((i / 3 + 2) % 3) * SLOT + (i % 3) * 4096); }
DEVI u32 cell(int i, int w) { return cell0(i) + (u32)w * 12288u; }
DEVI u32 scan_scr(int w) { return (u32)((w >> 1) * SLOT + TAIL + (w & 1) * 2048); }
DEVI u32 lds_peek(u32 a) { return *(volatile LAS u32*)(uintptr_t)a; }
DEVI u32 lds_word(u32 a) { return __builtin_amdgcn_readfirstlane(lds_peek(a)); }
DEVI void lds_post(u32 a, u32 v) {
  asm volatile("" ::: "memory");
  *(volatile LAS u32*)(uintptr_t)a = v;
  asm volatile("" ::: "memory");
}
DEVI void lds_spin_ge(u32 a, u32 v) {
#pragma unroll 1
  for (u32 spins = 0; spins < (SPIN_CAP << 4); spins++) {
    if (lds_word(a) >= v) break;
    __builtin_amdgcn_s_sleep(1);
  }
  asm volatile("" ::: "memory");
}

DEVI void all_four(u32 a) {
#pragma unroll 1
  for (u32 spins = 0; spins < (SPIN_CAP << 4); spins++) {
    const v4u d = *(volatile LAS v4u*)(uintptr_t)a;
    if (__builtin_amdgcn_readfirstlane(d.x & d.y & d.z & d.w)) break;
    __builtin_amdgcn_s_sleep(1);
  }
  asm volatile("" ::: "memory");
}

DEVI void glds16x4(u32 lds, u32 v0, u32 v1, u32 v2, u32 v3, v4u srd, u32 soff) {
  lds = __builtin_amdgcn_readfirstlane(lds);
  asm volatile("s_mov_b32 m0, %0\n\ts_nop 0\n\tbuffer_load_dwordx4 %1, %5, %6 offen lds\n\t"
               "buffer_load_dwordx4 %2, %5, %6 offen offset:1024 lds\n\t"
               "buffer_load_dwordx4 %3, %5, %6 offen offset:2048 lds\n\t"
               "buffer_load_dwordx4 %4, %5, %6 offen offset:3072 lds" ::"s"(lds), "v"(v0), "v"(v1), "v"(v2), "v"(v3), "s"(srd), "s"(soff)
               : "memory");
}
DEVI void glds16x2(u32 lds, u32 v0, u32 v1, v4u srd, u32 soff) {
  lds = __builtin_amdgcn_readfirstlane(lds);
  asm volatile("s_mov_b32 m0, %0\n\ts_nop 0\n\tbuffer_load_dwordx4 %1, %3, %4 offen lds\n\t"
               "buffer_load_dwordx4 %2, %3, %4 offen offset:1024 lds" ::"s"(lds), "v"(v0), "v"(v1), "s"(srd), "s"(soff)
               : "memory");
}
DEVI void split3(float v, u32& a, u32& b, u32& c) {
  const __bf16 x = (__bf16)v;
  const float r1 = v - (float)x;
  const __bf16 y = (__bf16)r1;
  const __bf16 z = (__bf16)(r1 - (float)y);
  a = __builtin_bit_cast(u16, x); b = __builtin_bit_cast(u16, y); c = __builtin_bit_cast(u16, z);
}

DEVI int p2_rt(int b) { const int bi = b >> 3; return (b & 7) * 4 + (bi == 0 ? 2 : bi == 1 ? 1 : bi == 19 || bi == 11 ? 0 : bi >> 3); }
DEVI int p2_ct(int b) { const int bi = b >> 3; return bi < 2 || bi == 31 ? 3 : bi == 19 ? 0 : bi == 11 ? 1 : bi == 27 ? 7 : bi & 7; }
template <int V> struct IC { static constexpr int value = V; };
template <int I, int N, class Fn> DEVI void static_for(Fn&& f) {
  if constexpr (I < N) { f(IC<I>{}); static_for<I + 1, N>(f); }
}
struct Ctx {
  int w, lane, pn, cg, ct;
  u32 tlo, thi, vf, vc, flag, sink;
};

DEVI u32 frag_xa(int lane, int ks) { return (u32)((lane & 31) * 128 + (((2 * ks + (lane >> 5)) ^ (((lane & 31) >> 1) & 7)) << 4)); }
DEVI u32 frag_wa(int lane, int wq) {
  const int g1 = (lane >> 4) & 1, a = (lane & 15) >> 2, pp = lane & 3;
  return ((pp >> 1) ? W3O : W1O) + (8 * (lane >> 5) + a) * 256 + (((4 * wq + g1) ^ (2 * a)) << 4) + 8 * (pp & 1);
}
DEVI constexpr u32 kb_of(int u) { return (u32)(u < 14 ? 2 * u : 2 * (u - 14) + 1); }
DEVI void stage_init(const Params& p, const Ctx& c, int g, int w) {
#pragma unroll
  for (int k = 0; k < 6; k++) glds16x2(cell(3 * g, w) + k * 2048, c.vf, c.vf + 7168, make_srd(p.w2, F * D * 2), kb_of(6 * g + k) * 16384u);
}

struct Start { v2u pe; v4u xr[2]; u32 plr, pli, pld; };
DEVI void start_loads(const Params& p, Ctx& c, Start& s, bool ssm) {
  const u32 co = ssm ? (u32)(c.cg * 128 + c.w * 32 + (c.lane & 31)) * 2 : OOB;
  const u32 ro = (u32)((blockIdx.x * 8 + c.w * 2) * 1024 + c.lane * 16);
  u64 pc;
  asm volatile("s_getpc_b64 %0" : "=s"(pc));
  const v4u st = make_srd((const void*)(pc & ~255ull), c.w >= 2 ? CODE_BYTES : 0u);
  const u32 to = (u32)((((blockIdx.x >> 3) & 3) * 2 + (c.w & 1)) * 8192 + c.lane * 128);
  asm volatile("buffer_load_ushort %3, %8, %11, 0 offen\n\tbuffer_load_ushort %4, %8, %12, 0 offen\n\tbuffer_load_ushort %5, %8, %13, 0 offen\n\t"
               "buffer_load_dwordx4 %0, %7, %9, 0 offen\n\tbuffer_load_dwordx4 %1, %7, %9, 0 offen offset:1024\n\t"
               "buffer_load_dwordx2 %2, off, %10, 0 sc1\n\tbuffer_load_dword %6, %14, %15, 0 offen"
               : "=&v"(s.xr[0]), "=&v"(s.xr[1]), "=&v"(s.pe), "=&v"(s.plr), "=&v"(s.pli), "=&v"(s.pld), "=&v"(c.sink)
               : "v"(ro), "v"(co), "s"(make_srd(p.x, L * D * 2)), "s"(make_srd(p.ws, WS_BYTES)), "s"(make_srd(p.lre, P * 2)),
                 "s"(make_srd(p.lim, P * 2)), "s"(make_srd(p.ldt, P * 2)), "v"(to), "s"(st)
               : "memory");
}
DEVI void start_wait(Start& s) {
  wait_vm<1>();
  asm volatile("" : "+v"(s.xr[0]), "+v"(s.xr[1]), "+v"(s.pe), "+v"(s.plr), "+v"(s.pli), "+v"(s.pld));
}
DEVI void start_jobs(const Params& p, Ctx& c, const Start& s, int kind) {
  const int lane = c.lane;
  const __amdgpu_buffer_rsrc_t rws = __builtin_amdgcn_make_buffer_rsrc(p.ws, 0, WS_BYTES, 0x00020000);
  float v[2][8], m[2], q[2];
#pragma unroll
  for (int k = 0; k < 2; k++) {
#pragma unroll
    for (int i = 0; i < 4; i++) { v[k][2 * i] = bf2f(s.xr[k][i]); v[k][2 * i + 1] = bfhi(s.xr[k][i]); }
    m[k] = wave_sum(((v[k][0] + v[k][1]) + (v[k][2] + v[k][3])) + ((v[k][4] + v[k][5]) + (v[k][6] + v[k][7]))) * (1.f / D);
  }
#pragma unroll
  for (int k = 0; k < 2; k++) {
    q[k] = 0.f;
#pragma unroll
    for (int i = 0; i < 8; i++) { const float d = v[k][i] - m[k]; q[k] = sfma(d, d, q[k]); }
    q[k] = wave_sum(q[k]);
  }
  const float ro = __builtin_amdgcn_rsqf((lane ? q[1] : q[0]) * (1.f / D) + 1e-5f);
  const u64 tag = (((u64)s.pe.x | ((u64)s.pe.y << 32)) * 0x9E3779B97F4A7C15ull + 0x632BE59BD9B4E019ull) | 1ull;
  c.tlo = __builtin_amdgcn_readfirstlane((u32)tag);
  c.thi = __builtin_amdgcn_readfirstlane((u32)(tag >> 32));
  lds_w<v2u>(TAGW, v2u{c.tlo, c.thi});
  if (lane < 2)
    __builtin_amdgcn_raw_buffer_store_b128(mkrec(lane ? m[1] : m[0], ro, c.tlo, c.thi), rws, WS_RS + (blockIdx.x * 8 + c.w * 2 + lane) * 16, 0, 16);
  const u32 fl = kind == 1 ? WS_FLF + (c.pn * 56 + c.cg * 4 + c.w) * 128 : kind == 0 ? WS_FLS + (c.pn * 16 + c.cg * 4 + c.w) * 128 : OOB;
  if (lane < 8) __builtin_amdgcn_raw_buffer_store_b128(v4u{0, 0, 0, 0}, rws, fl + lane * 16, 0, 16);
}
DEVI void read_tag(Ctx& c) {
  const v2u t = lds_r<v2u>(TAGW);
  c.tlo = __builtin_amdgcn_readfirstlane(t.x);
  c.thi = __builtin_amdgcn_readfirstlane(t.y);
}

template <int I, int N>
DEVI void prefetch(const Params& p, const Ctx& c, v4u (&R)[64]) {
#pragma unroll
  for (int i = I; i < I + N; i += 2) {
    const bool cw = i >= 32;
    const v4u srd = cw ? make_srd(i >= 48 ? p.cim : p.cre, D * P * 2) : make_srd(p.w2, F * D * 2);
    asm volatile("buffer_load_dwordx4 %0, %2, %4, %5 offen\n\tbuffer_load_dwordx4 %1, %3, %4, %5 offen"
                 : "=&v"(R[i]), "=&v"(R[i + 1])
                 : "v"(cw ? c.vc : c.vf), "v"(cw ? (c.vc ^ 64u) + 8192 : c.vf + 8192), "s"(srd),
                   "s"(cw ? (u32)(((i >> 3) & 1) * 128 + (i & 7) * 8192) : kb_of(12 + (i >> 1)) * 16384u)
                 : "memory");
  }
}
template <int I, int N, int PEND>
DEVI void settle(v4u (&R)[64]) {
  wait_vm<PEND>();
#pragma unroll
  for (int i = I; i < I + N; i += 2) asm volatile("" : "+v"(R[i]), "+v"(R[i + 1]));
}
struct Res { v2u dk, xv[4]; v4u st[4]; };
DEVI void res_issue(const Params& p, const Ctx& c, Res& r) {
  const int lt = (c.w & 3) * 64 + c.lane, dq = lt & 15, grow0 = p2_rt(blockIdx.x) * 64 + tau(lt >> 4);
  const u32 xo = (u32)(grow0 * 1024 + c.ct * 128 + dq * 8), so = WS_RS + grow0 * 16;
  asm volatile("buffer_load_dwordx2 %0, %9, %12, 0 offen\n\t"
               "buffer_load_dwordx2 %1, %10, %13, 0 offen\n\tbuffer_load_dwordx2 %2, %10, %13, %15 offen\n\t"
               "buffer_load_dwordx2 %3, %10, %13, %16 offen\n\tbuffer_load_dwordx2 %4, %10, %13, %17 offen\n\t"
               "buffer_load_dwordx4 %5, %11, %14, 0 offen sc1\n\tbuffer_load_dwordx4 %6, %11, %14, 0 offen offset:128 sc1\n\t"
               "buffer_load_dwordx4 %7, %11, %14, 0 offen offset:512 sc1\n\tbuffer_load_dwordx4 %8, %11, %14, 0 offen offset:640 sc1"
               : "=&v"(r.dk), "=&v"(r.xv[0]), "=&v"(r.xv[1]), "=&v"(r.xv[2]), "=&v"(r.xv[3]),
                 "=&v"(r.st[0]), "=&v"(r.st[1]), "=&v"(r.st[2]), "=&v"(r.st[3])
               : "v"((u32)(c.ct * 128 + dq * 8)), "v"(xo), "v"(so), "s"(make_srd(p.dsk, D * 2)), "s"(make_srd(p.x, L * D * 2)),
                 "s"(make_srd(p.ws, WS_BYTES)), "s"(8192u), "s"(32768u), "s"(40960u)
               : "memory");
}
template <int PEND>
DEVI void res_finish(const Params& p, const Ctx& c, Res& r, f32x4 (&o)[4]) {
  wait_vm<PEND>();
  asm volatile("" : "+v"(r.dk), "+v"(r.xv[0]), "+v"(r.xv[1]), "+v"(r.xv[2]), "+v"(r.xv[3]), "+v"(r.st[0]), "+v"(r.st[1]), "+v"(r.st[2]), "+v"(r.st[3]));
  auto good = [&]() {
    bool ok = true;
#pragma unroll
    for (int e = 0; e < 4; e++) ok = ok & recok(r.st[e], c.tlo, c.thi);
    return __all(ok);
  };
  if (!good()) {
    const __amdgpu_buffer_rsrc_t rws = __builtin_amdgcn_make_buffer_rsrc(p.ws, 0, WS_BYTES, 0x00020000);
    const int grow0 = p2_rt(blockIdx.x) * 64 + tau(((c.w & 3) * 64 + c.lane) >> 4);
#pragma unroll 1
    for (u32 spins = 0; spins < SPIN_CAP; spins++) {
#pragma unroll
      for (int e = 0; e < 4; e++) r.st[e] = __builtin_amdgcn_raw_buffer_load_b128(rws, WS_RS + (grow0 + (e & 1) * 8 + (e >> 1) * 32) * 16, 0, 16);
      if (good()) break;
      __builtin_amdgcn_s_sleep(4);
    }
  }
#pragma unroll
  for (int e = 0; e < 4; e++) {
    const float m = __uint_as_float(r.st[e].x), rs = __uint_as_float(r.st[e].z);
    const float x0 = bf2f(r.xv[e].x), x1 = bfhi(r.xv[e].x), x2 = bf2f(r.xv[e].y), x3 = bfhi(r.xv[e].y);
    o[e][0] = sfma(x0 - m, smul(bf2f(r.dk.x), rs), x0);
    o[e][1] = sfma(x1 - m, smul(bfhi(r.dk.x), rs), x1);
    o[e][2] = sfma(x2 - m, smul(bf2f(r.dk.y), rs), x2);
    o[e][3] = sfma(x3 - m, smul(bfhi(r.dk.y), rs), x3);
    asm volatile("" : "+v"(o[e]));
  }
}
DEVI void supply(const Params& p, const Ctx& c, v4u (&R)[64], bool hold) {
  const int lw = c.w & 3, lane = c.lane;
  const __amdgpu_buffer_rsrc_t rws = __builtin_amdgcn_make_buffer_rsrc(p.ws, 0, WS_BYTES, 0x00020000);
  const int rt = p2_rt(blockIdx.x);
  const int pna = (2 * rt * 205) >> 10, pnb = ((2 * rt + 1) * 205) >> 10;
  const bool isf = lane < 14 || (lane >= 16 && lane < 30 && pnb != pna);
  u32 fo = isf ? WS_FLF + (u32)((lane < 16 ? pna : pnb) * 56 + 14 * lw + (lane & 15)) * 128 : OOB;
  bool s0 = !isf;
  u32 lvl = 0;
  auto watch = [&]() {
    const v4u fl = __builtin_amdgcn_raw_buffer_load_b128(rws, fo, 0, 16);
    fo = (fl.z == c.tlo && fl.w == c.thi) ? OOB : fo;
    s0 = s0 | (fl.x == c.tlo && fl.y == c.thi) | (fo == OOB);
    lvl = __all(fo == OOB) ? 2u : __all(s0) ? 1u : 0u;
    lds_post(F_RDY + lw * 4, lvl);
  };
  lds_spin_ge(F_OUT + lw * 4, 1);
  if (hold) __builtin_amdgcn_s_sleep(WDELAY);
  static_for<1, 8>([&](auto E_) {
    constexpr int ev = decltype(E_)::value, lo = 12 + 8 * (ev - 1), n = ev < 7 ? 8 : 4;
#pragma unroll 1
    for (u32 spins = 0; spins < (SPIN_CAP << 3); spins++) {
      if (lds_word(F_CONS + lw * 4) >= (u32)ev) break;
      if (ev <= 2 && lvl < 2) watch();
      __builtin_amdgcn_s_sleep(POLLSLEEP);
    }
    if constexpr (lo + n > 52) settle<lo, n, 64 - lo - n>(R); else asm volatile("" ::: "memory");
#pragma unroll
    for (int k = 0; k < n; k++) lds_w<v4u>(cell((2 * (ev - 1) + (k >> 2)) % 9, lw) + (k & 3) * 1024 + lane * 16, R[lo + k]);
    lds_post(F_FILL + lw * 4, (u32)ev + 1);
    if constexpr (ev == LASTAT) {
      __builtin_amdgcn_s_setprio(3);
      prefetch<52, 12>(p, c, R);
      __builtin_amdgcn_s_setprio(0);
    }
  });
}

struct Ld { u32 vx[5], vw[4]; v4u sx, sw; int sig; };
DEVI void issue_group(const Ld& l, int lw, bool ssm, int g, u32 sbase, int parts = 3) {
  const u32 st = (u32)((g + l.sig) & 7);
  const u32 so = st * 128, sk = ssm ? so : st * (64 * F * 2);
  const u32 wd = ((lw >> 1) ? W3O : W1O) + (lw & 1) * 8192, xd = XO + lw * (ssm ? 4096 : 5120);
  if (parts & 1) {
    glds16x4(sbase + wd, l.vw[0], l.vw[1], l.vw[2], l.vw[3], l.sw, sk);
    glds16x4(sbase + wd + 4096, l.vw[0], l.vw[1], l.vw[2], l.vw[3], l.sw, sk + (ssm ? 32768 : 16 * F * 2));
    SBAR();
  }
  if (parts & 2) {
    glds16x4(sbase + xd, l.vx[0], l.vx[1], l.vx[2], l.vx[3], l.sx, so);
    if (!ssm) glds16(sbase + xd + 4096, l.vx[4], l.sx, so);
  }
}
DEVI void loader_addr(const Ctx& c, Ld& l, bool ssm) {
  const int lw = c.w - 4, lane = c.lane, cg = c.cg;
  const int r8 = lane >> 3, c8 = lane & 7;
  l.sig = 4 * (blockIdx.x & 1);
  const u32 va = (u32)((cg * 128 + (lw & 1) * 64 + r8) * 1024 + ((c8 ^ (r8 >> 1)) << 4));
  const u32 vb = (u32)(((lw & 1) * 32 + (lane >> 4)) * (F * 2) + cg * 256 + (((lane & 15) ^ ((lane >> 4) << 1)) << 4));
  const u32 v0 = ssm ? va : vb, v1 = ssm ? va ^ 64u : vb, str = ssm ? 7168u : (u32)(8 * F - 1024);
  l.vw[0] = v0; l.vw[1] = v1 + str; l.vw[2] = v0 + 2 * str; l.vw[3] = v1 + 3 * str;
}
DEVI void loader_start(u64 px, u64 pw, const Ctx& c, Ld& l, bool ssm) {
  const int lw = c.w - 4;
  l.sx = make_srd((const void*)px, L * D * 2);
  l.sw = make_srd((const void*)pw, ssm ? P * D * 2 : D * F * 2);
  issue_group(l, lw, ssm, 0, 0, 1);
  const int lane = c.lane, pn = c.pn, r8 = lane >> 3, c8 = lane & 7;
  const int mt = ssm ? 4 : 5;
  const u32 lo = (u32)((16 * (r8 >> 2) + (r8 & 3)) * 1024 + ((c8 ^ (r8 >> 1)) << 4));
#pragma unroll
  for (int e = 0; e < 5; e++) {
    const int q = mt * lw + e;
    l.vx[e] = (lo ^ (u32)((q & 1) << 6)) + (u32)((pn * 32 * mt + (q >> 2) * 32 + (q & 3) * 4 - (e & 3)) * 1024);
  }
  issue_group(l, lw, ssm, 0, 0, 2);
  issue_group(l, lw, ssm, 1, SLOT);
}

template <bool SSM>
DEVI void loader(const Params& p, Ctx& c, const Ld& l, f32x4 (&o)[4], v4u (&R)[64]) {
  constexpr int NG = SSM ? 12 : 13, MT = SSM ? 4 : 5, PA = SSM ? 0 : PFA, PB = SSM ? 0 : PFB;
  constexpr u32 RT_R = XC + MT * 512;
  const int lw = c.w - 4, lane = c.lane, pn = c.pn, cg = c.cg;
  const int sig = l.sig;
  const int cst = (pn >> 2) == (sig >> 1) ? (pn & 3) : -1;
  const int cgi = SSM ? 14 + cg : cg;
  const v4u sws = make_srd(p.ws, WS_BYTES);
  const __amdgpu_buffer_rsrc_t rws = __builtin_amdgcn_make_buffer_rsrc(p.ws, 0, WS_BYTES, 0x00020000);
  auto issue = [&](int g, u32 sbase) { issue_group(l, lw, SSM, g, sbase); };
  if (lane < 32) lds_w<u32>(ZERO + lane * 4, 0u);
  wait_vm_bar<NG>();
  read_tag(c);
  const u32 tlo = c.tlo, thi = c.thi;
  auto colsum = [&](u32 sbase, int stage) {
    __builtin_amdgcn_s_setprio(3);
    bf16x8 f[8];
#pragma unroll
    for (int ks = 0; ks < 4; ks++) {
      if constexpr (SSM) {
        f[ks] = lds_r<bf16x8>(sbase + W1O + lw * 4096 + frag_xa(lane, ks));
        f[4 + ks] = lds_r<bf16x8>(sbase + W3O + lw * 4096 + frag_xa(lane, ks));
      } else {
        const u32 a0 = sbase + frag_wa(lane, lw) + ks * 4096, a1 = a0 ^ 32u;
        f[ks] = lds_tr2(a0, a0 + 1024);
        f[4 + ks] = lds_tr2(a1, a1 + 1024);
      }
    }
    f32x4 sa[4] = {};
#pragma unroll
    for (int ks = 0; ks < 4; ks++) {
      sa[0] = sum4(__builtin_shufflevector(f[ks], f[ks], 0, 1, 2, 3), sa[0]);
      sa[1] = sum4(__builtin_shufflevector(f[ks], f[ks], 4, 5, 6, 7), sa[1]);
      sa[2] = sum4(__builtin_shufflevector(f[4 + ks], f[4 + ks], 0, 1, 2, 3), sa[2]);
      sa[3] = sum4(__builtin_shufflevector(f[4 + ks], f[4 + ks], 4, 5, 6, 7), sa[3]);
    }
    const float c0 = half_sum(sa[0][0] + sa[1][0]), c1 = half_sum(sa[2][0] + sa[3][0]);
    __builtin_amdgcn_s_setprio(0);
    const float n0 = dppx<0xB1>(c0), n1 = dppx<0xB1>(c1);
    __builtin_amdgcn_raw_buffer_store_b128(mkrec((lane & 1) ? n1 : c0, (lane & 1) ? c1 : n0, tlo, thi), rws,
        lane < 32 ? WS_CS + ((cgi * 8 + stage) * 128 + (2 * lw + (lane & 1)) * 16 + (lane >> 1)) * 16 : OOB, 0, 16);
  };
  const int lt = lw * 64 + lane;
  const int ti = lt - (256 - 32 * MT), grow = pn * (32 * MT) + ti;
  const bool hasc = lt < 128, hasr = ti >= 0 && grow < L;
  const u32 gco = hasc ? WS_CS + (cgi * 8 * 128 + lt) * 16 : OOB;
  const u32 gro = hasr ? WS_RS + grow * 16 : OOB;
  v4u gr[9];
  if constexpr (PA == 0) {
    u32 sbase = 0, pbase = 2 * SLOT;
#pragma unroll 1
    for (int s = 0; s < 5; s++) {
      if (cst == s) colsum(sbase, (s + sig) & 7);
      issue(s + 2, pbase);
      if (cst == s) wait_vm_bar<NG + 1>(); else wait_vm_bar<NG>();
      pbase = sbase;
      sbase = sbase == 2 * SLOT ? 0 : sbase + SLOT;
    }
  } else {
    static_for<0, 5>([&](auto S_) {
      constexpr int s = decltype(S_)::value;
      if (s < 4 && cst == s) colsum((s % 3) * SLOT, (s + sig) & 7);
      issue(s + 2, ((s + 2) % 3) * SLOT);
      if constexpr (s > 0) prefetch<PA * (s - 1), PA>(p, c, R);
      constexpr int N = NG + (s > 0 ? PA : 0) + (s > 1 ? PA : 0);
      if (s < 4 && cst == s) wait_vm_bar<N + 1>(); else wait_vm_bar<N>();
    });
  }
  issue(7, SLOT);
  prefetch<4 * PA, PA>(p, c, R);
  wait_vm_bar<PA + NG + PA>();
  asm volatile("buffer_load_dwordx4 %0, %9, %11, 0 offen sc1\n\tbuffer_load_dwordx4 %1, %9, %11, 0 offen offset:2048 sc1\n\t"
               "buffer_load_dwordx4 %2, %9, %11, %12 offen sc1\n\tbuffer_load_dwordx4 %3, %9, %11, %12 offen offset:2048 sc1\n\t"
               "buffer_load_dwordx4 %4, %9, %11, %13 offen sc1\n\tbuffer_load_dwordx4 %5, %9, %11, %13 offen offset:2048 sc1\n\t"
               "buffer_load_dwordx4 %6, %9, %11, %14 offen sc1\n\tbuffer_load_dwordx4 %7, %9, %11, %14 offen offset:2048 sc1\n\t"
               "buffer_load_dwordx4 %8, %10, %11, 0 offen sc1"
               : "=&v"(gr[0]), "=&v"(gr[1]), "=&v"(gr[2]), "=&v"(gr[3]), "=&v"(gr[4]), "=&v"(gr[5]), "=&v"(gr[6]), "=&v"(gr[7]), "=&v"(gr[8])
               : "v"(gco), "v"(gro), "s"(sws), "s"(4096u), "s"(8192u), "s"(12288u)
               : "memory");
  Res rs;
  res_issue(p, c, rs);
  stage_init(p, c, 0, lw);
  wait_vm<63 - PB>();
  prefetch<5 * PA, PB>(p, c, R);
  wait_vm_bar<PA + 18 + 12 + PB>();
  __builtin_amdgcn_s_setprio(3);
  wait_vm<9 + 12 + PB>();
  asm volatile("" : "+v"(gr[0]), "+v"(gr[1]), "+v"(gr[2]), "+v"(gr[3]), "+v"(gr[4]), "+v"(gr[5]), "+v"(gr[6]), "+v"(gr[7]), "+v"(gr[8]));
  {
    bool ok = !hasr || recok(gr[8], tlo, thi);
#pragma unroll
    for (int k = 0; k < 8; k++) ok = ok & (!hasc || recok(gr[k], tlo, thi));
    if (!__all(ok)) {
#pragma unroll 1
      for (u32 spins = 0; spins < SPIN_CAP; spins++) {
#pragma unroll
        for (int k = 0; k < 8; k++) gr[k] = __builtin_amdgcn_raw_buffer_load_b128(rws, gco + k * 2048, 0, 16);
        gr[8] = __builtin_amdgcn_raw_buffer_load_b128(rws, gro, 0, 16);
        ok = !hasr || recok(gr[8], tlo, thi);
#pragma unroll
        for (int k = 0; k < 8; k++) ok = ok & (!hasc || recok(gr[k], tlo, thi));
        if (__all(ok)) break;
        __builtin_amdgcn_s_sleep(2);
      }
    }
    if (hasc) {
      float a = 0.f, b = 0.f;
#pragma unroll
      for (int k = 0; k < 8; k++) { a += __uint_as_float(gr[k].x); b += __uint_as_float(gr[k].z); }
      u32 h0, m0, l0, h1, m1, l1;
      split3(a, h0, m0, l0);
      split3(b, h1, m1, l1);
      lds_w<v4u>(WC + lt * 32, v4u{h0 | (h0 << 16), m0 | (m0 << 16), h0 | (l0 << 16), 0u});
      lds_w<v4u>(WC + lt * 32 + 16, v4u{h1 | (h1 << 16), m1 | (m1 << 16), h1 | (l1 << 16), 0u});
    }
    if (ti >= 0) {
      const int rho = (ti & 3) | ((ti >> 2) & 4) | ((ti << 1) & 24);
      u32 hh, mm, ll;
      split3(-__uint_as_float(gr[8].x), hh, mm, ll);
      lds_w<v4u>(XC + ((ti & ~31) + rho) * 16, v4u{hh | (mm << 16), hh | (mm << 16), ll | (hh << 16), 0u});
      lds_w<u32>(RT_R + ti * 4, gr[8].z);
    }
  }
  lds_post(F_TAB + lw * 4, 1u);
  __builtin_amdgcn_s_setprio(0);
  stage_init(p, c, 1, lw);
  res_finish<12 + PB + 12>(p, c, rs, o);
  if constexpr (!SSM) {
    wait_vm<63 - (NPRE - 5 * PA - PB)>();
    prefetch<5 * PA + PB, NPRE - 5 * PA - PB>(p, c, R);
    settle<0, 12, (NPRE - 5 * PA - PB < NPRE - 12 ? NPRE - 5 * PA - PB : NPRE - 12)>(R);
    all_four(F_DONE);
#pragma unroll
    for (int k = 0; k < 12; k++) lds_w<v4u>(cell(6, lw) + k * 1024 + lane * 16, R[k]);
    lds_post(F_FILL + lw * 4, 1u);
    settle<12, NPRE - 12, 0>(R);
  } else {
    wait_vm<20>();
    prefetch<12, NPRE - 12>(p, c, R);
    all_four(F_DONE);
    wait_vm<40>();
    stage_init(p, c, 2, lw);
    settle<12, NPRE - 12, 0>(R);
    lds_post(F_FILL + lw * 4, 1u);
  }
}

template <int MT> struct Frag { bf16x8 x[MT]; bf16x8 wv[2]; };
template <bool SSM>
DEVI void compute(const Params& p, Ctx& c, Start& st0) {
  constexpr int MT = SSM ? 4 : 5, NM = 2 * MT;
  constexpr u32 RT_R = XC + MT * 512;
  const int w = c.w, lane = c.lane, pn = c.pn, cg = c.cg;
  const int l31 = lane & 31, h = lane >> 5;
  const __amdgpu_buffer_rsrc_t rws = __builtin_amdgcn_make_buffer_rsrc(p.ws, 0, WS_BYTES, 0x00020000);
  cf lam, s2, l16, l32, l128, l256, l512, pw[4];
  f32x16 acc[MT][2];
#pragma unroll
  for (int m = 0; m < MT; m++)
#pragma unroll
    for (int i = 0; i < 16; i++) { acc[m][0][i] = 0.f; acc[m][1][i] = 0.f; }
  u32 xa[4];
#pragma unroll
  for (int ks = 0; ks < 4; ks++) xa[ks] = frag_xa(lane, ks);
  const u32 wa0 = frag_wa(lane, w), wa1 = wa0 ^ 32u;
  SBAR();
  const u32 plr = st0.plr, pli = st0.pli, pld = st0.pld;
  if constexpr (SSM) {
    const float lr = bf2f(plr), li = bf2f(pli);
    const float dt = (float)(__bf16)expf(bf2f(pld));
    const float er = expf(dt * lr);
    float sn, cn;
    sincosf(dt * li, &sn, &cn);
    lam = cf{smul(er, cn), smul(er, sn)};
    const float inv = 2.f / sfma(lr, lr, smul(li, li));
    const cf nq = cmul(cf{lam.r - 1.f, lam.i}, cf{lr, -li});
    s2 = cf{smul(nq.r, inv), smul(nq.i, inv)};
    const cf l2 = cmul(lam, lam), l4 = cmul(l2, l2), l8 = cmul(l4, l4);
    l16 = cmul(l8, l8);
    l32 = cmul(l16, l16);
    const cf l64 = cmul(l32, l32);
    l128 = cmul(l64, l64); l256 = cmul(l128, l128); l512 = cmul(l256, l256);
    pw[0] = cmul(s2, cf{h ? l16.r : 1.f, h ? l16.i : 0.f});
    pw[1] = cmul(pw[0], l32); pw[2] = cmul(pw[0], l64); pw[3] = cmul(pw[1], l64);
    asm volatile("" : "+v"(l128.r), "+v"(l128.i), "+v"(l256.r), "+v"(l256.i), "+v"(l512.r), "+v"(l512.i), "+v"(pw[0].r), "+v"(pw[0].i),
                 "+v"(pw[1].r), "+v"(pw[1].i), "+v"(pw[2].r), "+v"(pw[2].i), "+v"(pw[3].r), "+v"(pw[3].i));

  }
  const u32 tlo = c.tlo, thi = c.thi;

  auto mm = [&](const Frag<MT>& f, int i) {
    const int m = i >> 1, n = (i ^ m) & 1;
    if constexpr (SSM) acc[m][n] = mfma(f.x[m], f.wv[n], acc[m][n]);
    else acc[m][n] = mfma(f.wv[n], f.x[m], acc[m][n]);
  };
  auto rd = [&](Frag<MT>& f, int q, u32 sb_, int ks) {
    const u32 ax = sb_ + xa[ks];
    if constexpr (SSM) {
      const u32 ab = ax + w * 4096;
      if (q == 0) { f.x[0] = lds_r<bf16x8>(ax); f.wv[0] = lds_r<bf16x8>(ab + W1O); }
      if (q == 1) { f.wv[1] = lds_r<bf16x8>(ab + W3O); f.x[1] = lds_r<bf16x8>(ax + 4096); }
      if (q == 2) { f.x[2] = lds_r<bf16x8>(ax + 8192); f.x[3] = lds_r<bf16x8>(ax + 12288); }
    } else {
      const u32 a0 = sb_ + wa0 + ks * 4096, a1 = sb_ + wa1 + ks * 4096;
      if (q == 0) f.wv[0] = lds_tr2(a0, a0 + 1024);
      if (q == 1) { f.x[0] = lds_r<bf16x8>(ax); f.x[1] = lds_r<bf16x8>(ax + 4096); }
      if (q == 2) f.wv[1] = lds_tr2(a1, a1 + 1024);
      if (q == 3) { f.x[2] = lds_r<bf16x8>(ax + 8192); f.x[3] = lds_r<bf16x8>(ax + 12288); }
      if (q == 4) f.x[MT - 1] = lds_r<bf16x8>(ax + 16384);
    }
  };
  auto step = [&](const Frag<MT>& cur, Frag<MT>& nxt, u32 sb_, int ks) {
#pragma unroll
    for (int i = 0; i < NM; i++) {
      mm(cur, i);
      SBAR();
      if (i < 5) { rd(nxt, i, sb_, ks); SBAR(); }
    }
  };

  Frag<MT> fa, fb;
  lds_bar();
#pragma unroll
  for (int q = 0; q < 5; q++) rd(fa, q, 0, 0);
  u32 sbase = 0;
  SBAR();
  step(fa, fb, 0, 1);
#pragma unroll 1
  for (int s = 0; s < 7; s++) {
    SBAR();
    step(fb, fa, sbase, 2);
    step(fa, fb, sbase, 3);
    sbase = sbase == 2 * SLOT ? 0 : sbase + SLOT;
    mm(fb, 0);
    mm(fb, 1);
    SBAR();
    asm volatile("s_waitcnt vmcnt(0) lgkmcnt(0)\n\ts_barrier" ::"v"(c.sink) : "memory");
#pragma unroll
    for (int i = 2; i < NM; i++) {
      if (i < 7) { rd(fa, i - 2, sbase, 0); SBAR(); }
      mm(fb, i);
      SBAR();
    }
    step(fa, fb, sbase, 1);
  }
  SBAR();
  step(fb, fa, SLOT, 2);
  step(fa, fb, SLOT, 3);
  lds_post(F_DONE + w * 4, 1u);
  const v4u tb = *(volatile LAS v4u*)(uintptr_t)F_TAB;
  SBAR();
  mm(fb, 0);
  mm(fb, 1);
  SBAR();
  if (!__builtin_amdgcn_readfirstlane(tb.x & tb.y & tb.z & tb.w)) all_four(F_TAB);
  asm volatile("" ::: "memory");
  const u32 cx = h ? ZERO : XC + l31 * 16, cxs = h ? 0u : 512u;
  f32x4 r4[SSM ? 16 : 1];
  float rr[MT];
#pragma unroll
  for (int i = 2; i < NM; i++) {
    if (i == 2) { fa.wv[0] = lds_r<bf16x8>(WC + w * 1024 + l31 * 16); fa.x[0] = lds_r<bf16x8>(cx); }
    if (i == 3) { fa.wv[1] = lds_r<bf16x8>(WC + w * 1024 + 512 + l31 * 16); fa.x[1] = lds_r<bf16x8>(cx + cxs); }
    if (i == 4) { fa.x[2] = lds_r<bf16x8>(cx + 2 * cxs); fa.x[3] = lds_r<bf16x8>(cx + 3 * cxs); }
    if constexpr (!SSM) {
      if (i == 5) fa.x[MT - 1] = lds_r<bf16x8>(cx + 4 * cxs);
      if (i == 6) {
#pragma unroll
        for (int m = 0; m < MT; m++) rr[m] = lds_r<float>(RT_R + (u32)(m * 32 + tau(l31)) * 4);
      }
    }
    SBAR();
    mm(fb, i);
    SBAR();
  }
  if constexpr (!SSM) {
#pragma unroll
    for (int m = 0; m < MT; m++) { acc[m][0] = mfma(fa.wv[0], fa.x[m], acc[m][0]); SBAR(); }
    f32x2 ke[MT], iv[MT];
    u32 ho[MT];
#pragma unroll
    for (int m = 0; m < MT; m++) {
      ke[m] = dup(rr[m] * -1.4426950408889634f);
      iv[m] = dup(__builtin_amdgcn_rcpf(rr[m] * rr[m]));
      ho[m] = WS_H + lane * 16 + ((pn * 5 + m) * 176 + cg * 8 + w * 2) * 1024;
    }
#pragma unroll
    for (int n = 0; n < 2; n++)
#pragma unroll
      for (int m = 0; m < MT; m++) {
        const f32x16& a = acc[m][n];
        u32 o[4];
        f32x2 g[4], t[4];
#pragma unroll
        for (int j = 0; j < 4; j++) { g[j] = f32x2{a[8 * (j >> 1) + 2 * (j & 1)], a[8 * (j >> 1) + 2 * (j & 1) + 1]}; t[j] = g[j] * ke[m]; }
        SBAR();
        if (n == 0) { acc[m][1] = mfma(fa.wv[1], fa.x[m], acc[m][1]); SBAR(); }
#pragma unroll
        for (int j = 0; j < 4; j++) t[j] = f32x2{__builtin_amdgcn_exp2f(t[j].x), __builtin_amdgcn_exp2f(t[j].y)};
        SBAR();
#pragma unroll
        for (int j = 0; j < 2; j++) g[j] = g[j] * f32x2{a[4 + 2 * j], a[5 + 2 * j]};
        SBAR();
#pragma unroll
        for (int j = 0; j < 4; j++) t[j] = fma2(t[j], iv[m], iv[m]);
        SBAR();
#pragma unroll
        for (int j = 0; j < 4; j++) t[j] = f32x2{__builtin_amdgcn_rcpf(t[j].x), __builtin_amdgcn_rcpf(t[j].y)};
        SBAR();
#pragma unroll
        for (int j = 2; j < 4; j++) g[j] = g[j] * f32x2{a[8 + 2 * j], a[9 + 2 * j]};
        SBAR();
#pragma unroll
        for (int j = 0; j < 4; j++) g[j] = g[j] * t[j];
        SBAR();
#pragma unroll
        for (int j = 0; j < 4; j++) o[j] = pk2(g[j].x, g[j].y);
        __builtin_amdgcn_raw_buffer_store_b128(v4u{o[0], o[1], o[2], o[3]}, rws, ho[m] + n * 1024, 0, 16);
        if (n == 1 && m == HALFAT) {
          wait_vm<HALFAT + 1>();
          if (lane == 0) __builtin_amdgcn_raw_buffer_store_b64(v2u{tlo, thi}, rws, WS_FLF + (pn * 56 + cg * 4 + w) * 128, 0, 16);
          lds_post(F_OUT + w * 4, 1u);
        }
        SBAR();
      }
    c.flag = WS_FLF + (pn * 56 + cg * 4 + w) * 128 + 8;
  } else {
#pragma unroll
    for (int i = 0; i < NM; i++) {
      mm(fa, i);
      SBAR();
      r4[2 * i] = lds_r<f32x4>(RT_R + (u32)((i & 1) * 64 + h * 16 + (i >> 1) * 4) * 4);
      r4[2 * i + 1] = lds_r<f32x4>(RT_R + (u32)((i & 1) * 64 + 32 + h * 16 + (i >> 1) * 4) * 4);
      SBAR();
    }
    asm volatile("s_nop 7\n\ts_nop 3");
    SBAR();
    const f32x2 ar = dup(lam.r), ai = dup(lam.i), an = dup(-lam.i);
    f32x2 ur[2][16], ui[2][16], er[2], ei[2];
    auto form = [&](int k, int i) {
      const float ra = r4[(i >> 2) * 4 + 2 * k][i & 3], rb = r4[(i >> 2) * 4 + 2 * k + 1][i & 3];
      ur[k][i] = f32x2{smul(acc[2 * k][0][i], ra), smul(acc[2 * k + 1][0][i], rb)};
      ui[k][i] = f32x2{smul(acc[2 * k][1][i], ra), smul(acc[2 * k + 1][1][i], rb)};
    };
    form(0, 0); form(1, 0);
    er[0] = ur[0][0]; ei[0] = ui[0][0]; er[1] = ur[1][0]; ei[1] = ui[1][0];
    form(0, 1); form(1, 1);
    SBAR();
#pragma unroll
    for (int i = 1; i < 16; i++) {
      const f32x2 a0 = fma2(an, ei[0], ur[0][i]), b0 = fma2(ai, er[0], ui[0][i]), a1 = fma2(an, ei[1], ur[1][i]), b1 = fma2(ai, er[1], ui[1][i]);
      SBAR();
      if (i < 15) form(0, i + 1);
      SBAR();
      er[0] = fma2(ar, er[0], a0); ei[0] = fma2(ar, ei[0], b0); er[1] = fma2(ar, er[1], a1); ei[1] = fma2(ar, ei[1], b1);
      SBAR();
      if (i < 15) form(1, i + 1);
      SBAR();
    }
    cf lo[4], hi2[4], zm[5];
#pragma unroll
    for (int m = 0; m < 4; m++) half_both(cf{er[m >> 1][m & 1], ei[m >> 1][m & 1]}, lo[m], hi2[m]);
#pragma unroll
    for (int m = 0; m < 4; m++) hi2[m] = cfma(l16, lo[m], hi2[m]);
    zm[0] = cf{0.f, 0.f};
    zm[1] = hi2[0];
#pragma unroll
    for (int m = 1; m < 4; m++) zm[m + 1] = cfma(l32, zm[m], hi2[m]);
    if (h == 0)
      __builtin_amdgcn_raw_buffer_store_b128(mkrec(zm[4].r, zm[4].i, tlo, thi), rws, WS_AGG + (cg * 128 + w * 32 + l31) * 16 + pn * 8192, 0, 16);
    SBAR();
    const f32x2 sr = dup(s2.r), si = dup(s2.i), sn = dup(-s2.i);
    auto scale = [&](int k) {
#pragma unroll
      for (int i = 0; i < 16; i += 2) {
        const f32x2 a0 = sn * ui[k][i], b0 = si * ur[k][i], a1 = sn * ui[k][i + 1], b1 = si * ur[k][i + 1];
        SBAR();
        ur[k][i] = fma2(sr, ur[k][i], a0); ui[k][i] = fma2(sr, ui[k][i], b0);
        ur[k][i + 1] = fma2(sr, ur[k][i + 1], a1); ui[k][i + 1] = fma2(sr, ui[k][i + 1], b1);
        asm volatile("" : "+v"(ur[k][i]), "+v"(ui[k][i]), "+v"(ur[k][i + 1]), "+v"(ui[k][i + 1]));
        SBAR();
      }
    };
    cf zs[4];
#pragma unroll
    for (int m = 0; m < 4; m++) {
      const cf z1 = cfma(l16, zm[m], lo[m]);
      const float zr = h ? z1.r : zm[m].r, zi = h ? z1.i : zm[m].i;
      zs[m] = cmul(s2, cf{zr, zi});
    }
    asm volatile("" : "+v"(zs[0].r), "+v"(zs[0].i), "+v"(zs[1].r), "+v"(zs[1].i), "+v"(zs[2].r), "+v"(zs[2].i), "+v"(zs[3].r), "+v"(zs[3].i));
    scale(0);
    SBAR();
    const int j0 = pn - 1 - h;
    const u32 gb = (u32)(WS_AGG + (cg * 128 + w * 32 + l31) * 16 + j0 * 8192);
    v4u t[8];
    auto gather = [&]() {
#pragma unroll
      for (int q = 0; q < 8; q++) t[q] = __builtin_amdgcn_raw_buffer_load_b128(rws, j0 >= 2 * q ? gb - q * 16384 : OOB, 0, 16);
    };
    auto complete = [&]() {
      u32 bad = 0;
#pragma unroll
      for (int q = 0; q < 8; q++) {
        const u32 d = (t[q].y ^ t[q].z ^ tlo) | (t[q].w ^ t[q].x ^ thi);
        bad |= j0 >= 2 * q ? d : 0u;
      }
      return __all(bad == 0u);
    };
    gather();
    SBAR();
    scale(1);
    all_four(F_DONE);
    SBAR();
    if (!complete()) {
#pragma unroll 1
      for (u32 spins = 0; spins < SPIN_CAP; spins++) {
        gather();
        if (complete()) break;
        __builtin_amdgcn_s_sleep(2);
      }
    }
    cf g2[4], S0, S1;
#pragma unroll
    for (int q = 0; q < 4; q++)
      g2[q] = cfma(l256, cf{__uint_as_float(t[2 * q + 1].x), __uint_as_float(t[2 * q + 1].z)}, cf{__uint_as_float(t[2 * q].x), __uint_as_float(t[2 * q].z)});
    half_both(cfma(l512, cfma(l512, cfma(l512, g2[3], g2[2]), g2[1]), g2[0]), S0, S1);
    const cf C = cfma(l128, S1, S0);
    const u32 scr = scan_scr(w);
    const u32 fsw = (l31 >> 1) & 3;
    const u32 wb0 = scr + l31 * 64 + (((2 * h) ^ fsw) << 4), wb1 = wb0 ^ 16u;
    const int g1 = (lane >> 4) & 1, a = (lane & 15) >> 2, pp = lane & 3;
    const int cki = 2 * (pp & 1) + g1;
    const u32 r0 = scr + (8 * h + a) * 64 + ((cki ^ (a >> 1)) << 4) + 8 * (pp >> 1);
    const u32 r1 = scr + (8 * h + 4 + a) * 64 + ((cki ^ (2 + (a >> 1))) << 4) + 8 * (pp >> 1);
    const u32 ho = WS_H + lane * 16 + (pn * 4 * 176 + 112 + cg * 8 + w * 2) * 1024;
    f32x2 hr[2], hi[2];
#pragma unroll
    for (int k = 0; k < 2; k++) {
      const cf ha = cfma(pw[2 * k], C, zs[2 * k]), hb = cfma(pw[2 * k + 1], C, zs[2 * k + 1]);
      hr[k] = f32x2{ha.r, hb.r}; hi[k] = f32x2{ha.i, hb.i};
    }
    lds_post(F_OUT + w * 4, 1u);
    u32 pk[8][8];
    SBAR();
#pragma unroll
    for (int i = 0; i < 16; i++) {
      const f32x2 a0 = fma2(an, hi[0], ur[0][i]), b0 = fma2(ai, hr[0], ui[0][i]), a1 = fma2(an, hi[1], ur[1][i]), b1 = fma2(ai, hr[1], ui[1][i]);
      SBAR();
      hr[0] = fma2(ar, hr[0], a0); hi[0] = fma2(ar, hi[0], b0); hr[1] = fma2(ar, hr[1], a1); hi[1] = fma2(ar, hi[1], b1);
      ur[0][i] = hr[0]; ui[0][i] = hi[0]; ur[1][i] = hr[1]; ui[1][i] = hi[1];
      SBAR();
      if (i >= 2) {
        const int k = i & 1, j = (i - 2) & ~1;
        pk[4 * k][j >> 1] = pk2(ur[k][j].x, ur[k][j + 1].x); pk[4 * k + 2][j >> 1] = pk2(ur[k][j].y, ur[k][j + 1].y);
        pk[4 * k + 1][j >> 1] = pk2(-ui[k][j].x, -ui[k][j + 1].x); pk[4 * k + 3][j >> 1] = pk2(-ui[k][j].y, -ui[k][j + 1].y);
        SBAR();
      }
    }
#pragma unroll
    for (int k = 0; k < 2; k++) {
      pk[4 * k][7] = pk2(ur[k][14].x, ur[k][15].x); pk[4 * k + 2][7] = pk2(ur[k][14].y, ur[k][15].y);
      pk[4 * k + 1][7] = pk2(-ui[k][14].x, -ui[k][15].x); pk[4 * k + 3][7] = pk2(-ui[k][14].y, -ui[k][15].y);
    }
    SBAR();
    bf16x8 tv[16];
#pragma unroll
    for (int j = 0; j < 10; j++) {
      if (j < 8) {
        lds_w<v4u>(wb0, v4u{pk[j][0], pk[j][1], pk[j][2], pk[j][3]});
        lds_w<v4u>(wb1, v4u{pk[j][4], pk[j][5], pk[j][6], pk[j][7]});
        tv[2 * j] = lds_tr2(r0, r1);
        tv[2 * j + 1] = lds_tr2(r0 + 1024, r1 + 1024);
      }
      if (j >= 2) {
#pragma unroll
        for (int q = 0; q < 2; q++)
          __builtin_amdgcn_raw_buffer_store_b128(__builtin_bit_cast(v4u, tv[2 * (j - 2) + q]), rws,
                                                 ho + (((j - 2) >> 1) * 176 + ((j - 2) & 1) * 32 + q) * 1024, 0, 16);
      }
      SBAR();
    }
    c.flag = WS_FLS + (pn * 16 + cg * 4 + w) * 128;
  }
  SBAR();
}

constexpr int issued_before(int st) {
  int n = AINIT;
  for (int k = 0; k < st; k++)
    for (int j = 0; j < 2; j++)
      if (n < 44 && n <= k + AWIN && (n < 14 || k >= ODDAT) && (n < 28 || k >= SSMAT)) n++;
  return n;
}
DEVI void p2_compute(const Params& p, const Ctx& c, int rt) {
  const int w = c.w & 3, lane = c.lane, l31 = lane & 31, h = lane >> 5;
  const __amdgpu_buffer_rsrc_t rws = __builtin_amdgcn_make_buffer_rsrc(p.ws, 0, WS_BYTES, 0x00020000);
  const int g1 = (lane >> 4) & 1, a = (lane & 15) >> 2, pp = lane & 3;
  const u32 fa0 = (u32)((4 * h + a) * 128 + ((4 * (a >> 1) + 2 * g1 + (pp >> 1)) << 4) + 8 * (pp & 1)) + w * 12288, fa1 = fa0 ^ 64u;
  const u32 ca = (u32)(l31 * 128 + ((h ^ ((l31 >> 1) & 7)) << 4)) + w * 12288;
  const u32 hb = WS_H + lane * 16;
  const u32 hf0 = (u32)((2 * rt * 176 + 28 * w) * 1024), hs0 = (u32)((2 * rt * 176 + 112 + 8 * w) * 1024);
  constexpr int NST = 44;
  bf16x8 Af[NST][2], Bf[NST][2];
  auto lda1 = [&](int st, int mi) {
    const u32 kb = st < 28 ? kb_of(st) : ((st - 28) >> 3) * 32 + ((st - 28) & 7);
    const u32 so = (st < 28 ? hf0 : hs0) + (kb & ~3u) * 1024 + mi * 176 * 1024;
    Af[st][mi] = __builtin_bit_cast(bf16x8, __builtin_amdgcn_raw_buffer_load_b128(rws, hb + (kb & 3u) * 1024, so, 16));
  };
  auto lda = [&](int st) { lda1(st, 0); lda1(st, 1); };
  auto ldb = [&](int st) {
    if (st >= 28) {
      const int q = (st - 28) >> 2;
      const u32 o = ca ^ (u32)((st & 3) << 5);
      Bf[st][0] = lds_r<bf16x8>(o + cell0((5 + 2 * q) % 9));
      Bf[st][1] = lds_r<bf16x8>(o + cell0((6 + 2 * q) % 9));
    } else {
      const u32 base = cell0((st >> 1) % 9) + (st & 1) * 2048;
      Bf[st][0] = lds_tr2(fa0 + base, fa0 + base + 1024);
      Bf[st][1] = lds_tr2(fa1 + base, fa1 + base + 1024);
    }
  };
  u32 own = lane == 0 ? c.flag : OOB;
  if (lds_word(F_RDY + w * 4) < 1u) {
    wait_vm<0>();
    __builtin_amdgcn_raw_buffer_store_b64(v2u{c.tlo, c.thi}, rws, own, 0, 16);
    own = OOB;
    lds_spin_ge(F_RDY + w * 4, 1);
  }
  asm volatile("" ::: "memory");
#pragma unroll
  for (int st = 0; st < AINIT; st++) lda(st);
  wait_vm<2 * AINIT>();
  __builtin_amdgcn_raw_buffer_store_b64(v2u{c.tlo, c.thi}, rws, own, 0, 16);
  f32x16 acc[2][2];
#pragma unroll
  for (int k = 0; k < 16; k++) { acc[0][0][k] = 0.f; acc[0][1][k] = 0.f; acc[1][0][k] = 0.f; acc[1][1][k] = 0.f; }
  lds_spin_ge(F_FILL + w * 4, 1);
  ldb(0);
  u32 pk = 0;
  v2u sf = {0, 0};
  const u32 sfo = lane < 4 ? WS_FLS + (u32)((rt >> 1) * 16 + 4 * w + lane) * 128 : OOB;
  SBAR();
  static_for<0, NST>([&](auto ST_) {
    constexpr int st = decltype(ST_)::value;
    if constexpr (st == 17 || st == 25 || st == 31 || st == 35 || st == 39) {
      const u32 need = st == 17 ? 3u : st == 25 ? 5u : st == 31 ? 6u : st == 35 ? 7u : 8u;
      if (__builtin_amdgcn_readfirstlane(pk) < need) lds_spin_ge(F_FILL + w * 4, need);
      asm volatile("" ::: "memory");
    }
    if constexpr (st == ODDAT) {
      if (__builtin_amdgcn_readfirstlane(pk) < 2u) lds_spin_ge(F_RDY + w * 4, 2);
      asm volatile("" ::: "memory");
    }
    if constexpr (st == ODDAT - 1) pk = lds_peek(F_RDY + w * 4);
    if constexpr (st == SSMAT) {
      if (!__all(lane >= 4 || (sf.x == c.tlo && sf.y == c.thi))) {
#pragma unroll 1
        for (u32 spins = 0; spins < SPIN_CAP; spins++) {
          const v2u t = __builtin_amdgcn_raw_buffer_load_b64(rws, sfo, 0, 16);
          if (__all(lane >= 4 || (t.x == c.tlo && t.y == c.thi))) break;
          __builtin_amdgcn_s_sleep(POLLSLEEP);
        }
      }
      asm volatile("" ::: "memory");
    }
    if constexpr (st == 16 || st == 24 || st == 30 || st == 34 || st == 38) pk = lds_peek(F_FILL + w * 4);
    if constexpr (st == SFAT) sf = __builtin_amdgcn_raw_buffer_load_b64(rws, sfo, 0, 16);
    constexpr int l0 = issued_before(st), l1 = issued_before(st + 1), la = l1 > l0 ? l0 : -1, lb = l1 > l0 + 1 ? l0 + 1 : -1;
    acc[0][0] = mfma(Bf[st][0], Af[st][0], acc[0][0]);
    SBAR();
    if constexpr (st + 1 < NST) ldb(st + 1);
    SBAR();
    acc[0][1] = mfma(Bf[st][1], Af[st][0], acc[0][1]);
    SBAR();
    if constexpr ((st & 3) == 3 && st < 28) lds_post(F_CONS + w * 4, (u32)(st >> 2) + 1);
    if constexpr (la >= 0) lda1(la, 0);
    SBAR();
    acc[1][1] = mfma(Bf[st][1], Af[st][1], acc[1][1]);
    SBAR();
    if constexpr (lb >= 0) lda1(la, 1);
    SBAR();
    acc[1][0] = mfma(Bf[st][0], Af[st][1], acc[1][0]);
    SBAR();
    if constexpr (lb >= 0) lda(lb);
    else if constexpr (la >= 0) lda1(la, 1);
    SBAR();
  });
#pragma unroll
  for (int mi = 0; mi < 2; mi++)
#pragma unroll
    for (int cc = 0; cc < 2; cc++)
#pragma unroll
      for (int j = 0; j < 4; j++) {
        const u32 pos = (u32)(lane ^ (cc * 8 + j * 2 + h));
        lds_w<f32x4>(mi * SLOT + (cc * 4 + j) * 1024 + w * 12288 + pos * 16,
                     f32x4{acc[mi][cc][4 * j], acc[mi][cc][4 * j + 1], acc[mi][cc][4 * j + 2], acc[mi][cc][4 * j + 3]});
      }
  lds_bar();
}

DEVI void p2_finish(const Params& p, const Ctx& c, const f32x4 (&o)[4], int rt) {
  const int lw = c.w & 3, lane = c.lane, ct = c.ct;
  const u32 tlo = c.tlo, thi = c.thi;
  const __amdgpu_buffer_rsrc_t rws = __builtin_amdgcn_make_buffer_rsrc(p.ws, 0, WS_BYTES, 0x00020000);
  const int lt = lw * 64 + lane, dq = lt & 15;
  const int grow0 = rt * 64 + tau(lt >> 4);
  lds_bar();
  if (blockIdx.x == 255 && lt == 0)
    __builtin_amdgcn_raw_buffer_store_b64(v2u{tlo, thi}, rws, WS_EPOCH, 0, 16);
#pragma unroll
  for (int e = 0; e < 4; e++) {
    const int rr = (lt >> 4) + 16 * e, rho = rr & 31;
    const u32 ad = (u32)((e >> 1) * SLOT + (dq >> 1) * 1024 + ((rho + 32 * (dq & 1)) ^ dq) * 16);
    const f32x4 s = (lds_r<f32x4>(ad) + lds_r<f32x4>(ad + 12288)) + (lds_r<f32x4>(ad + 24576) + lds_r<f32x4>(ad + 36864));
    __builtin_amdgcn_raw_buffer_store_b128(__builtin_bit_cast(v4u, s + o[e]), __builtin_amdgcn_make_buffer_rsrc(p.out, 0, L * D * 4, 0x00020000),
                                           (u32)(((grow0 + (e & 1) * 8 + (e >> 1) * 32) * D + ct * 64 + dq * 4) * 4), 0, 16);
  }
}

__global__ __launch_bounds__(512, 2) void fused(Params p) {
  const int w = __builtin_amdgcn_readfirstlane(threadIdx.x >> 6);
  u64 ka, kb;
  u32 kc;
  asm volatile("s_load_dwordx2 %0, %3, 0x0\n\ts_load_dwordx2 %1, %3, %4\n\ts_load_dword %2, %3, 0x40"
               : "=&s"(ka), "=&s"(kb), "=&s"(kc)
               : "s"(__builtin_amdgcn_kernarg_segment_ptr()), "s"((u32)((blockIdx.x < 64 ? 24 : 72) + ((w >> 1) & 1) * 8)));
  __shared__ __attribute__((aligned(16))) char smem[LDS_BYTES];
  asm volatile("" ::"s"((u32)(uintptr_t)(LAS char*)smem));
  const int b = blockIdx.x, xcd = b & 7, bi = b >> 3;
  const int lane = threadIdx.x & 63;
  const int r8 = lane >> 3, c8 = lane & 7;
  Ctx c;
  c.w = w; c.lane = lane; c.flag = OOB;
  int kind = 2;
  if (bi < 8) {
    kind = 0;
    c.pn = (xcd & 1) * 8 + bi; c.cg = xcd >> 1;
  } else {
    const int f = (xcd >> 1) * 23 + bi - 8, hh = xcd & 1;
    const int n0 = hh ? 6 : 7, sp = 7 * n0;
    const int f2 = f < sp ? f : f - sp, nn = f < sp ? n0 : 13 - n0;
    const int q = nn == 7 ? (f2 * 37) >> 8 : (f2 * 43) >> 8;
    c.cg = (f < sp ? 0 : 7) + q;
    c.pn = f2 - q * nn + (hh ? 13 - nn : 0);
    if (bi < 31 && f < 91) kind = 1;
  }
  Ld l;
  Start st0;
  if (w < 4) {
    asm volatile("s_waitcnt lgkmcnt(0)" ::"s"(ka), "s"(kb), "s"(kc) : "memory");
    start_loads(p, c, st0, kind == 0);
    start_wait(st0);
    start_jobs(p, c, st0, kind);
  } else {
    loader_addr(c, l, kind == 0);
    asm volatile("" : "+v"(l.vw[0]), "+v"(l.vw[1]), "+v"(l.vw[2]), "+v"(l.vw[3]));
    u64 px, pw;
    asm volatile("s_waitcnt lgkmcnt(0)\n\ts_mov_b64 %0, %2\n\ts_mov_b64 %1, %3" : "=&s"(px), "=&s"(pw) : "s"(ka), "s"(kb), "s"(kc) : "memory");
    if (kind != 2) loader_start(px, pw, c, l, kind == 0);
  }
  int b2 = b;
  asm volatile("" : "+s"(b2) :: "memory");
  const int rt = p2_rt(b2), ct = p2_ct(b2);
  c.ct = ct;
  c.vf = (u32)((448 * (w & 3) + r8) * 1024 + ct * 128 + ((c8 ^ (((r8 >> 1) & 1) << 2)) << 4));
  c.vc = (u32)((64 * ct + r8) * 1024 + (w & 3) * 256 + ((c8 ^ (r8 >> 1)) << 4));
  if (w >= 4) {
    f32x4 o[4];
    v4u R[64];
    if (kind == 1) loader<false>(p, c, l, o, R);
    else if (kind == 0) loader<true>(p, c, l, o, R);
    else {
      if (lane < 32) lds_w<u32>(ZERO + lane * 4, 0u);
      lds_bar();
      read_tag(c);
      lds_spin_ge(F_OUT + (w - 4) * 4, 1);
      stage_init(p, c, 0, w - 4); stage_init(p, c, 1, w - 4); stage_init(p, c, 2, w - 4);
      Res rs;
      res_issue(p, c, rs);
      res_finish<0>(p, c, rs, o);
      lds_post(F_FILL + (w - 4) * 4, 1u);
      prefetch<12, NPRE - 12>(p, c, R);
      settle<12, NPRE - 12, 0>(R);
    }
    supply(p, c, R, kind == 1);
    p2_finish(p, c, o, rt);
  } else {
    __builtin_amdgcn_s_setprio(2);
    if (kind == 1) compute<false>(p, c, st0);
    else if (kind == 0) compute<true>(p, c, st0);
    else {
      asm volatile("s_waitcnt vmcnt(0) lgkmcnt(0)\n\ts_barrier" ::"v"(c.sink) : "memory");
      {
        const int si = (bi == 31 ? xcd : 8 + (xcd & 1)) * 4 + w;
        const __amdgpu_buffer_rsrc_t r2 = __builtin_amdgcn_make_buffer_rsrc((void*)p.w2, 0, F * D * 2, 0x00020000);
        const __amdgpu_buffer_rsrc_t rc = __builtin_amdgcn_make_buffer_rsrc((void*)((si & 1) ? p.cim : p.cre), 0, D * P * 2, 0x00020000);
        u32 a = 0;
#pragma unroll
        for (int i = 0; i < 6; i++) a ^= __builtin_amdgcn_raw_buffer_load_b32(r2, (u32)((i * 40 + si) * 8192 + lane * 128), 0, 0);
#pragma unroll
        for (int i = 0; i < 4; i++) a ^= __builtin_amdgcn_raw_buffer_load_b32(rc, (u32)((i * 20 + (si >> 1)) * 8192 + lane * 128), 0, 0);
        asm volatile("" ::"v"(a));
      }
      lds_post(F_OUT + w * 4, 1u);
    }
    __builtin_amdgcn_s_setprio(1);
    p2_compute(p, c, rt);
  }
}

extern "C" void kernel_launch(void* const* d_in, const int* in_sizes, int n_in, void* d_out, int out_size, void* d_ws,
                              size_t ws_size, hipStream_t stream) {
  Params p;
  p.x = (const u16*)d_in[0];
  p.lre = (const u16*)d_in[1];
  p.lim = (const u16*)d_in[2];
  p.bre = (const u16*)d_in[3];
  p.bim = (const u16*)d_in[4];
  p.cre = (const u16*)d_in[5];
  p.cim = (const u16*)d_in[6];
  p.ldt = (const u16*)d_in[7];
  p.dsk = (const u16*)d_in[8];
  p.w1 = (const u16*)d_in[10];
  p.w3 = (const u16*)d_in[11];
  p.w2 = (const u16*)d_in[12];
  p.out = (float*)d_out;
  p.ws = (char*)d_ws;
  fused<<<256, 512, 0, stream>>>(p);
}
```
